```python
import torch

def _softmax(x, axis=-1):
    return torch.softmax(x, dim=axis)

def _gelu(x):
    return torch.nn.functional.gelu(x, approximate='tanh')

def _silu(x):
    return torch.nn.functional.silu(x)

def _sigmoid(x):
    return torch.sigmoid(x)

def _layernorm(x, axis=-1, eps=1e-05):
    m = x.mean(dim=axis, keepdim=True)
    v = x.var(dim=axis, keepdim=True, unbiased=False)
    return (x - m) / torch.sqrt(v + eps)

def _scatter_add(tgt, idx, src, axis=0):
    return tgt.clone().index_add_(axis, idx, src)

def _segment_sum(src, seg, n):
    out = torch.zeros((n,) + src.shape[1:], dtype=src.dtype)
    return out.index_add_(0, seg, src)

def _pad(x, pw):
    flat = tuple((v for pair in reversed(pw) for v in pair))
    return torch.nn.functional.pad(x, flat)
kernel_name = 'gated_mix__memory_aug_recurrent_mem_tr__dense_activation_squared_relu__prompt'

def setup_inputs():

    _w = lambda a: (lambda t: t.to(torch.bfloat16) if t.is_floating_point() else t)(torch.as_tensor(a))

    def _A_setup():
        B, S, Dm = (1, 2048, 512)
        H, Dh = (32, 128)
        M, seg = (8, 32)
        seg = min(seg, S)
        S = S // seg * seg
        return {'x': _w((lambda *s: torch.randn(*s, dtype=torch.bfloat16) * 0.05)(B, S, Dm)), 'mem0': _w((lambda *s: torch.randn(*s, dtype=torch.bfloat16) * 0.8)(B, M, Dm)), 'Wq': _w((lambda *s: torch.randn(*s, dtype=torch.bfloat16) * 1.6)(Dm, H, Dh) * Dm ** (-0.5)), 'Wk': _w((lambda *s: torch.randn(*s, dtype=torch.bfloat16) * 1.6)(Dm, H, Dh) * Dm ** (-0.5)), 'Wv': _w((lambda *s: torch.randn(*s, dtype=torch.bfloat16) * 0.8)(Dm, H, Dh) * Dm ** (-0.5)), 'Wo': _w((lambda *s: torch.randn(*s, dtype=torch.bfloat16) * 0.8)(H, Dh, Dm) * (H * Dh) ** (-0.5)), 'seg_len': seg}
    _A_in = _A_setup()

    def _B_setup():
        B, S, D, F = (1, 2048, 512, 1792)
        return {'x': _w((lambda *s: torch.randn(*s, dtype=torch.bfloat16) * 0.05)(B, S, D)), 'w1': _w((lambda *s: torch.randn(*s, dtype=torch.bfloat16) * 0.05)(D, F)), 'b1': _w((lambda *s: torch.randn(*s, dtype=torch.bfloat16) * 0.1)(F)), 'w2': _w((lambda *s: torch.randn(*s, dtype=torch.bfloat16) * 0.05)(F, D)), 'b2': _w((lambda *s: torch.randn(*s, dtype=torch.bfloat16) * 0.05)(D))}
    _B_in = _B_setup()
    return {'A_x': _A_in['x'], 'A_mem0': _A_in['mem0'], 'A_Wq': _A_in['Wq'], 'A_Wk': _A_in['Wk'], 'A_Wv': _A_in['Wv'], 'A_Wo': _A_in['Wo'], 'A_seg_len': _A_in['seg_len'], 'B_x': _B_in['x'], 'B_w1': _B_in['w1'], 'B_b1': _B_in['b1'], 'B_w2': _B_in['w2'], 'B_b2': _B_in['b2'], 'gate_w': _w((lambda *s: torch.randn(*s, dtype=torch.bfloat16) * 0.05)(512) * 16.0)}

def reference(A_x, A_mem0, A_Wq, A_Wk, A_Wv, A_Wo, A_seg_len, B_x, B_w1, B_b1, B_w2, B_b2, gate_w):

    def _A_ref(x, mem0, Wq, Wk, Wv, Wo, seg_len):
        B, S, Dm = x.shape
        H, Dh = (Wq.shape[1], Wq.shape[2])
        M = mem0.shape[1]
        scale = Dh ** (-0.5)
        n_seg = S // seg_len
        L = M + seg_len
        idx = torch.arange(L)
        mask = (idx[:, None] < M) | (idx[:, None] >= idx[None, :])
        mem = mem0
        outs = []
        for s in range(n_seg):
            xs = x[:, s * seg_len:(s + 1) * seg_len, :].clone()
            h = torch.concatenate([mem, xs], axis=1)
            q = torch.einsum('btd,dhe->bhte', h, Wq)
            k = torch.einsum('btd,dhe->bhte', h, Wk)
            v = torch.einsum('btd,dhe->bhte', h, Wv)
            scores = torch.einsum('bhqe,bhke->bhqk', q, k) * scale
            scores = torch.where(mask[None, None], scores, -1000000000.0)
            p = _softmax(scores, axis=-1)
            o = torch.einsum('bhqk,bhke->bhqe', p, v)
            o = torch.einsum('bhte,hed->btd', o, Wo)
            mem = o[:, :M, :].clone()
            outs.append(o[:, M:, :])
        return torch.concatenate(outs, axis=1)

    def _B_ref(x, w1, b1, w2, b2):
        h = torch.einsum('bsd,df->bsf', x, w1) + b1
        zero = torch.zeros_like(h)
        r = torch.where(h > zero, h, zero)
        h = r * r
        out = torch.einsum('bsf,fd->bsd', h, w2) + b2
        return out
    x = A_x
    g = _sigmoid((x * gate_w).sum(-1, keepdims=True))
    a = _A_ref(x, A_mem0, A_Wq, A_Wk, A_Wv, A_Wo, A_seg_len)
    b = _B_ref(x, B_w1, B_b1, B_w2, B_b2)
    return g * a + (1 - g) * b
```

```cpp
#include <hip/hip_runtime.h>
#include <hip/hip_bf16.h>
#include <stdint.h>

typedef __attribute__((ext_vector_type(2))) unsigned u32x2;
typedef __attribute__((ext_vector_type(4))) unsigned u32x4;
typedef __attribute__((ext_vector_type(8))) unsigned u32x8;
typedef __attribute__((ext_vector_type(2))) float f32x2;
typedef __attribute__((ext_vector_type(4))) float f32x4;
typedef __attribute__((ext_vector_type(2))) __bf16 bf16x2;
typedef __attribute__((ext_vector_type(8))) __bf16 bf16x8;
typedef __attribute__((ext_vector_type(16))) __bf16 bf16x16;
typedef __attribute__((ext_vector_type(4))) short s16x4;
typedef __amdgpu_buffer_rsrc_t rsrc_t;

#define D1 10
#define D2 7
#define POLA 0
#define POLB 0

#define SC1 16
#define DEVI static __device__ __forceinline__

#define OFF_FLAGS 0u
#define OFF_MEM   16384u
#define OFF_PART  (2u << 20)
#define OFF_KX    (4u << 20)
#define OFF_VX    (20u << 20)
#define OFF_KVM   (36u << 20)
#define OFF_OF    (44u << 20)
#define OFF_HF    (60u << 20)
#define OFF_G     (68u << 20)
#define WS_BYTES  (69u << 20)
#define FL_K 0
#define FL_V 32
#define FL_R 64
#define FL_O 128
#define FL_H 160
#define FL_G 192
#define SPIN_MAX (1 << 17)

DEVI rsrc_t mkrsrc(const void* p, unsigned bytes) { return __builtin_amdgcn_make_buffer_rsrc((void*)p, 0, bytes, 0x00020000); }
DEVI u32x4 ld128(rsrc_t r, unsigned v, unsigned s) { return __builtin_amdgcn_raw_buffer_load_b128(r, v, s, 0); }
DEVI u32x2 ld64(rsrc_t r, unsigned v, unsigned s) { return __builtin_amdgcn_raw_buffer_load_b64(r, v, s, 0); }
DEVI unsigned ld32(rsrc_t r, unsigned v, unsigned s) { return __builtin_amdgcn_raw_buffer_load_b32(r, v, s, 0); }
DEVI u32x4 ld128c(rsrc_t r, unsigned v, unsigned s) { return __builtin_amdgcn_raw_buffer_load_b128(r, v, s, SC1); }
DEVI u32x2 ld64c(rsrc_t r, unsigned v, unsigned s) { return __builtin_amdgcn_raw_buffer_load_b64(r, v, s, SC1); }
DEVI unsigned ld32c(rsrc_t r, unsigned v, unsigned s) { return __builtin_amdgcn_raw_buffer_load_b32(r, v, s, SC1); }
DEVI void st128c(u32x4 d, rsrc_t r, unsigned v, unsigned s) { __builtin_amdgcn_raw_buffer_store_b128(d, r, v, s, SC1); }
DEVI void st64c(u32x2 d, rsrc_t r, unsigned v, unsigned s) { __builtin_amdgcn_raw_buffer_store_b64(d, r, v, s, SC1); }
DEVI void st32c(unsigned d, rsrc_t r, unsigned v, unsigned s) { __builtin_amdgcn_raw_buffer_store_b32(d, r, v, s, SC1); }
DEVI unsigned pk(float a, float b) { f32x2 v = {a, b}; return __builtin_bit_cast(unsigned, __builtin_convertvector(v, bf16x2)); }
DEVI unsigned plo(unsigned a, unsigned b) { return __builtin_amdgcn_perm(b, a, 0x05040100u); }
DEVI unsigned phi(unsigned a, unsigned b) { return __builtin_amdgcn_perm(b, a, 0x07060302u); }
DEVI float asf(unsigned x) { return __builtin_bit_cast(float, x); }
DEVI unsigned asu(float x) { return __builtin_bit_cast(unsigned, x); }
DEVI f32x4 smf(u32x4 a, u32x8 b, f32x4 c, int idx) {
  return __builtin_amdgcn_smfmac_f32_16x16x64_bf16(__builtin_bit_cast(bf16x8, a), __builtin_bit_cast(bf16x16, b), c, idx, 0, 0);
}
DEVI f32x4 mf32(u32x4 a, u32x4 b, f32x4 c) {
  return __builtin_amdgcn_mfma_f32_16x16x32_bf16(__builtin_bit_cast(bf16x8, a), __builtin_bit_cast(bf16x8, b), c, 0, 0, 0);
}
DEVI f32x4 mf16(u32x2 a, u32x2 b, f32x4 c) {
  return __builtin_amdgcn_mfma_f32_16x16x16bf16_1k(__builtin_bit_cast(s16x4, a), __builtin_bit_cast(s16x4, b), c, 0, 0, 0);
}
#define SMF "v_smfmac_f32_16x16x64_bf16 "
#define MF32 "v_mfma_f32_16x16x32_bf16 "
DEVI void smf4a(f32x4& c0, f32x4& c1, f32x4& c2, f32x4& c3, unsigned& mx, u32x4 a, const u32x8& b0, const u32x8& b1, const u32x8& b2, const u32x8& b3, int idx) {
  asm volatile("s_nop 1\n\t" SMF "%0, %5, %6, %10\n\t" SMF "%1, %5, %7, %10\n\tv_max3_u32 %4, %4, %11, %12\n\t"
               SMF "%2, %5, %8, %10\n\tv_max3_u32 %4, %4, %13, %14\n\t" SMF "%3, %5, %9, %10"
               : "+v"(c0), "+v"(c1), "+v"(c2), "+v"(c3), "+v"(mx)
               : "v"(a), "a"(b0), "a"(b1), "a"(b2), "a"(b3), "v"(idx), "v"(a[0]), "v"(a[1]), "v"(a[2]), "v"(a[3]));
}
DEVI void smf4v(f32x4& c0, f32x4& c1, f32x4& c2, f32x4& c3, u32x4 a, const u32x8& b0, const u32x8& b1, const u32x8& b2, const u32x8& b3, int idx) {
  asm volatile("s_nop 1\n\t" SMF "%0, %4, %5, %9\n\t" SMF "%1, %4, %6, %9\n\t" SMF "%2, %4, %7, %9\n\t" SMF "%3, %4, %8, %9"
               : "+v"(c0), "+v"(c1), "+v"(c2), "+v"(c3) : "v"(a), "v"(b0), "v"(b1), "v"(b2), "v"(b3), "v"(idx));
}
DEVI void smf2a(f32x4& c0, f32x4& c1, u32x4 a, const u32x8& b0, const u32x8& b1, int idx) {
  asm volatile("s_nop 1\n\t" SMF "%0, %2, %3, %5\n\t" SMF "%1, %2, %4, %5" : "+v"(c0), "+v"(c1) : "v"(a), "a"(b0), "a"(b1), "v"(idx));
}
DEVI void smf2v(f32x4& c0, f32x4& c1, u32x4 a, const u32x8& b0, const u32x8& b1, int idx) {
  asm volatile("s_nop 1\n\t" SMF "%0, %2, %3, %5\n\t" SMF "%1, %2, %4, %5" : "+v"(c0), "+v"(c1) : "v"(a), "v"(b0), "v"(b1), "v"(idx));
}
DEVI void smf1v(f32x4& c, u32x4 a, const u32x8& b, int idx) {
  asm volatile("s_nop 1\n\t" SMF "%0, %1, %2, %3" : "+v"(c) : "v"(a), "v"(b), "v"(idx));
}
DEVI void sc3_first(f32x4& s0, f32x4& s1, f32x4& sm, u32x4 k0, u32x4 k1, u32x4 f) {
  asm volatile("s_nop 1\n\t" MF32 "%0, %3, %5, 0\n\t" MF32 "%1, %4, %5, 0\n\t" MF32 "%2, %5, %5, 0"
               : "=&v"(s0), "=&v"(s1), "=&v"(sm) : "v"(k0), "v"(k1), "v"(f));
}
DEVI void sc3_next(f32x4& s0, f32x4& s1, f32x4& sm, u32x4 k0, u32x4 k1, u32x4 f) {
  asm volatile("s_nop 1\n\t" MF32 "%0, %3, %5, %0\n\t" MF32 "%1, %4, %5, %1\n\t" MF32 "%2, %5, %5, %2"
               : "+v"(s0), "+v"(s1), "+v"(sm) : "v"(k0), "v"(k1), "v"(f));
}
DEVI void mf32_acc(f32x4& c, unsigned& mx, u32x4 a, u32x4 b) {
  asm volatile("s_nop 1\n\t" MF32 "%0, %2, %3, %0\n\tv_max3_u32 %1, %1, %4, %5\n\tv_max3_u32 %1, %1, %6, %7"
               : "+v"(c), "+v"(mx) : "v"(a), "v"(b), "v"(a[0]), "v"(a[1]), "v"(a[2]), "v"(a[3]));
}
DEVI void pv2(f32x4& o0, f32x4& o1, u32x4 vx, u32x4 px, u32x2 vm, u32x2 pm) {
  asm volatile("s_nop 1\n\t" MF32 "%0, %2, %3, 0\n\tv_mfma_f32_16x16x16_bf16 %1, %4, %5, 0"
               : "=&v"(o0), "=&v"(o1) : "v"(vx), "v"(px), "v"(vm), "v"(pm));
}
DEVI void mwait(f32x4& a) { asm volatile("s_nop 7" : "+v"(a)); }
DEVI void mwait(f32x4& a, f32x4& b) { asm volatile("s_nop 7" : "+v"(a), "+v"(b)); }
DEVI void mwait(f32x4& a, f32x4& b, f32x4& c) { asm volatile("s_nop 7" : "+v"(a), "+v"(b), "+v"(c)); }
DEVI void mwait(f32x4& a, f32x4& b, f32x4& c, f32x4& d) { asm volatile("s_nop 7" : "+v"(a), "+v"(b), "+v"(c), "+v"(d)); }
DEVI void pin_a(u32x8& x) { asm volatile("" : "+a"(x)); }
DEVI void pin_v(u32x8& x) { asm volatile("" : "+v"(x)); }
DEVI float hsum(float a, float b) { u32x2 r = __builtin_amdgcn_permlane32_swap(asu(a), asu(b), false, false); return asf(r[0]) + asf(r[1]); }
DEVI f32x4 hsum4(f32x4 a, f32x4 b) {
  #pragma unroll
  for (int q = 0; q < 4; ++q) { u32x2 r = __builtin_amdgcn_permlane32_swap(asu(a[q]), asu(b[q]), false, false); a[q] = asf(r[0]); b[q] = asf(r[1]); }
  return a + b;
}
DEVI float vmax3(float a, float b, float c) { float d; asm("v_max3_f32 %0, %1, %2, %3" : "=v"(d) : "v"(a), "v"(b), "v"(c)); return d; }
DEVI float max12_g(f32x4 a, f32x4 b, f32x4 c) {
  float m = vmax3(vmax3(a[0], a[1], a[2]), vmax3(a[3], b[0], b[1]), vmax3(b[2], b[3], c[0])), t;
  asm("v_max3_f32 %0, %0, %2, %3\n\tv_max_f32 %0, %0, %4\n\tv_mov_b32 %1, %0\n\ts_nop 1\n\tv_permlane32_swap_b32 %0, %1\n\tv_max_f32 %0, %0, %1\n\t"
      "v_mov_b32 %1, %0\n\ts_nop 1\n\tv_permlane16_swap_b32 %0, %1\n\tv_max_f32 %0, %0, %1"
      : "+v"(m), "=&v"(t) : "v"(c[1]), "v"(c[2]), "v"(c[3]));
  return m;
}
DEVI unsigned lau(unsigned x) { asm volatile("" : "+v"(x)); return x; }
DEVI f32x4 zero4() {
  f32x2 a, b;
  asm volatile("v_mov_b64 %0, 0\n\tv_mov_b64 %1, 0" : "=v"(a), "=v"(b));
  return (f32x4){a[0], a[1], b[0], b[1]};
}
DEVI float red_max_g(float v) {
  u32x2 r = __builtin_amdgcn_permlane32_swap(asu(v), asu(v), false, false); v = fmaxf(asf(r[0]), asf(r[1]));
  r = __builtin_amdgcn_permlane16_swap(asu(v), asu(v), false, false); return fmaxf(asf(r[0]), asf(r[1]));
}
DEVI float red_sum_g(float v) {
  u32x2 r = __builtin_amdgcn_permlane32_swap(asu(v), asu(v), false, false); v = asf(r[0]) + asf(r[1]);
  r = __builtin_amdgcn_permlane16_swap(asu(v), asu(v), false, false); return asf(r[0]) + asf(r[1]);
}
DEVI unsigned umax3(unsigned a, unsigned b, unsigned c) { unsigned m = a > b ? a : b; return m > c ? m : c; }
DEVI unsigned guard(unsigned x) { return x < 0xFFFFFFFEu ? x : 0xFFFFFFFEu; }
DEVI void bar() { asm volatile("s_waitcnt lgkmcnt(0)\n\ts_barrier" ::: "memory"); }
DEVI void vm0() { asm volatile("s_waitcnt vmcnt(0)" ::: "memory"); }
DEVI unsigned rfl(unsigned x) { return __builtin_amdgcn_readfirstlane(x); }
DEVI int wait_flags(rsrc_t rw, int first, int n, int need, int lane, int& dead) {
  unsigned off = (unsigned)(first + (lane < n ? lane : 0)) * 4u;
  int it = 0;
  while (true) {
    int v = (int)ld32c(rw, off, OFF_FLAGS);
    asm volatile("" ::: "memory");
    if (__builtin_amdgcn_ballot_w64(v < need) == 0ull) {
      int m = v;
      #pragma unroll
      for (int o = 1; o < 32; o <<= 1) { int t = __shfl_xor(m, o); m = t < m ? t : m; }
      return (int)rfl((unsigned)m);
    }
    if (dead || ++it > SPIN_MAX) { dead = 1; return need; }
    __builtin_amdgcn_s_sleep(2);
  }
}

template <bool B> struct BoolC { static constexpr bool value = B; };
struct Params {
  const uint16_t* x; const uint16_t* mem0; const uint16_t* wq; const uint16_t* wk; const uint16_t* wv; const uint16_t* wo;
  const uint16_t* w1; const uint16_t* b1; const uint16_t* w2; const uint16_t* b2; const uint16_t* gw;
  uint16_t* out; char* ws;
};

#define SM_SCALE 0.12751743f

DEVI void role_rec(const Params& p, int r, char* smem) {
  const int tid = threadIdx.x, l = tid & 63, w = (int)rfl(tid >> 6);
  const int h = r >> 1, eh = r & 1;
  const int n = l & 15, gb = l >> 4;
  rsrc_t rw = mkrsrc(p.ws, WS_BYTES);
  const unsigned polA = POLA * 8192u, polB = POLB * 8192u;
  int dead = 0;
  {
    u32x4 ones = {~0u, ~0u, ~0u, ~0u};
    #pragma unroll
    for (int i = 0; i < 4; ++i) st128c(ones, rw, (unsigned)tid * 16u + (i & 1) * 4096u, OFF_PART + polA + ((i >> 1) * 64u + r) * 16384u);
    #pragma unroll
    for (int i = 0; i < 2; ++i) {
      int s = 1 + (tid >> 3) + 32 * i;
      if (s < 64) st128c(ones, rw, (unsigned)s * 16384u + (tid & 7) * 16u, OFF_MEM + polB + r * 128u);
    }
  }
  u32x8 Wqk[4][8], Wv[8], Wo[8];
  const unsigned rowl = 4 * (gb >> 1) + (gb & 1);
  {
    rsrc_t rv = mkrsrc(p.wv, 512u * 4096u * 2u);
    const unsigned vo = rowl * 8192u + (h * 128 + 64 * eh + 4 * n) * 2u;
    #pragma unroll
    for (int jj = 0; jj < 2; ++jj) {
      u32x2 a[8], b[8];
      #pragma unroll
      for (int m = 0; m < 8; ++m) {
        unsigned ro = (unsigned)(64 * (2 * w + jj) + 32 * (m >> 2) + 16 * ((m >> 1) & 1) + 2 * (m & 1)) * 8192u;
        a[m] = ld64(rv, vo, ro); b[m] = ld64(rv, vo, ro + 8u * 8192u);
      }
      #pragma unroll
      for (int t = 0; t < 4; ++t) {
        #pragma unroll
        for (int q = 0; q < 2; ++q) {
          u32x4 o;
          #pragma unroll
          for (int i = 0; i < 4; ++i) { int m = 4 * q + i; o[i] = (t & 1) ? phi(a[m][t >> 1], b[m][t >> 1]) : plo(a[m][t >> 1], b[m][t >> 1]); }
          *(u32x4*)(smem + 8192 + ((t * 8 + 2 * w + jj) * 2 + q) * 1024 + l * 16) = o;
        }
      }
    }
  }
  vm0();
  bar();
  if (w == 0 && l < 32 && n < 8) {
    int i = 4 * gb + (n >> 1), cc = n & 1;
    int db = 128 * (r >> 4) + 32 * ((r >> 2) & 3) + 4 * ((r >> 1) & 1) + 2 * (r & 1) + cc;
    const uint16_t* mp = p.mem0 + i * 512 + db;
    u32x2 v = {guard((unsigned)mp[0] | ((unsigned)mp[8] << 16)), guard((unsigned)mp[16] | ((unsigned)mp[24] << 16))};
    st64c(v, rw, 64u * gb + 8u * n, OFF_MEM + polB + r * 128u);
  }
  {
    rsrc_t rq = mkrsrc(w < 2 ? p.wq : p.wk, 512u * 4096u * 2u);
    const unsigned vo = rowl * 8192u + (h * 128 + 64 * (w & 1) + 4 * n) * 2u;
    #pragma unroll
    for (int j = 0; j < 8; ++j) {
      u32x2 a[8], b[8];
      #pragma unroll
      for (int m = 0; m < 8; ++m) {
        unsigned ro = (unsigned)(64 * j + 32 * (m >> 2) + 16 * ((m >> 1) & 1) + 2 * (m & 1)) * 8192u;
        a[m] = ld64(rq, vo, ro); b[m] = ld64(rq, vo, ro + 8u * 8192u);
      }
      #pragma unroll
      for (int m = 0; m < 8; ++m) {
        Wqk[0][j][m] = plo(a[m][0], b[m][0]); Wqk[1][j][m] = phi(a[m][0], b[m][0]);
        Wqk[2][j][m] = plo(a[m][1], b[m][1]); Wqk[3][j][m] = phi(a[m][1], b[m][1]);
      }
      pin_a(Wqk[0][j]); pin_a(Wqk[1][j]); pin_a(Wqk[2][j]); pin_a(Wqk[3][j]);
      if (j & 1) __builtin_amdgcn_sched_barrier(0);
    }
  }
  {
    rsrc_t ro_ = mkrsrc(p.wo, 4096u * 512u * 2u);
    const unsigned vo = (unsigned)(h * 128 + 64 * eh + 16 * (gb & 1) + (gb >> 1)) * 1024u + (128 * w + 8 * n) * 2u;
    #pragma unroll
    for (int q = 0; q < 2; ++q) {
      u32x4 a[4], b[4];
      #pragma unroll
      for (int i = 0; i < 4; ++i) {
        int m = 4 * q + i;
        unsigned ro = (unsigned)(32 * (m & 1) + 8 * ((m >> 1) & 1) + 2 * (m >> 2)) * 1024u;
        a[i] = ld128(ro_, vo, ro); b[i] = ld128(ro_, vo, ro + 4096u);
      }
      #pragma unroll
      for (int i = 0; i < 4; ++i)
        #pragma unroll
        for (int t = 0; t < 8; ++t) Wo[t][4 * q + i] = (t & 1) ? phi(a[i][t >> 1], b[i][t >> 1]) : plo(a[i][t >> 1], b[i][t >> 1]);
    }
  }
  __builtin_amdgcn_sched_barrier(0);
  #pragma unroll
  for (int j = 0; j < 8; ++j) {
    u32x4 a = *(const u32x4*)(smem + 8192 + ((w * 8 + j) * 2) * 1024 + l * 16);
    u32x4 b = *(const u32x4*)(smem + 8192 + ((w * 8 + j) * 2 + 1) * 1024 + l * 16);
    Wv[j] = (u32x8){a[0], a[1], a[2], a[3], b[0], b[1], b[2], b[3]};
    pin_v(Wv[j]); pin_v(Wo[j]);
  }
  const int idx = (l & 8) ? 0xEEEE : 0x4444;
  const unsigned l16 = (unsigned)l * 16u;
  const int hi = gb >> 1, g1 = gb & 1;
  char* const qimg_w = smem + ((w >> 1) * 8 + 4 * g1) * 288 + 128 * (n & 1) + 64 * hi + 32 * (w & 1) + 16 * (n >> 3) + 4 * ((n >> 1) & 3);
  const char* const qimg_r = smem + n * 288 + gb * 16;
  char* const oimg_w = smem + 4608 + n * 192 + 32 * w + 8 * gb;
  const char* const oimg_r = smem + 4608 + (l & 7) * 192 + (l >> 3) * 16;
  const unsigned pst = (unsigned)(16 * w + 4 * (n >> 2) + 2 * hi) * 16384u + r * 128u + (4 * g1 + (n & 3)) * 16u;
  const unsigned kvb = (unsigned)h * 64u;
  int kvp = wait_flags(rw, FL_K + h, 1, 0, l, dead);
  { int t = wait_flags(rw, FL_V + h, 1, 0, l, dead); kvp = t < kvp ? t : kvp; }
  u32x4 Kx[8], Vx;
  #pragma unroll
  for (int i = 0; i < 8; ++i) Kx[i] = ld128c(rw, l16 + (i & 3) * 1024u, OFF_KX + kvb * 8192u + (i >> 2) * 4096u);
  Vx = ld128c(rw, l16, OFF_VX + kvb * 8192u + (4 * eh + w) * 1024u);

  auto step = [&](const int s, auto last_c) {
    constexpr bool LAST = decltype(last_c)::value;
    u32x4 A[8];
    f32x4 acc[4];
    const unsigned mo = OFF_MEM + polB + (unsigned)s * 16384u;
    int it = 0;
    while (true) {
      const unsigned lb = lau(l16);
      #pragma unroll
      for (int j = 0; j < 8; ++j) A[j] = ld128c(rw, lb + (j & 3) * 1024u, mo + (j >> 2) * 4096u);
      __builtin_amdgcn_sched_barrier(0);
      unsigned mx = 0;
      #pragma unroll
      for (int t = 0; t < 4; ++t) acc[t] = zero4();
      #pragma unroll
      for (int j = 0; j < 8; ++j) smf4a(acc[0], acc[1], acc[2], acc[3], mx, A[j], Wqk[0][j], Wqk[1][j], Wqk[2][j], Wqk[3][j], idx);
      mwait(acc[0], acc[1], acc[2], acc[3]);
      asm volatile("" ::: "memory");
      if (__builtin_amdgcn_ballot_w64(mx == 0xFFFFFFFFu) == 0ull) break;
      if (dead || ++it > SPIN_MAX) { dead = 1; break; }
    }
    {
      f32x4 t0 = hsum4(acc[0], acc[2]), t1 = hsum4(acc[1], acc[3]);
      #pragma unroll
      for (int q = 0; q < 4; ++q) *(unsigned*)(qimg_w + q * 288) = pk(t0[q], t1[q]);
    }
    f32x4 av = zero4();
    bar();
    u32x4 F[4];
    #pragma unroll
    for (int k = 0; k < 4; ++k) F[k] = *(const u32x4*)(qimg_r + k * 64);
    int fk = 0;
    if (!LAST && kvp < 63) fk = (int)ld32c(rw, (lau(l16) & 16u) * 8u, OFF_FLAGS + (unsigned)h * 4u);
    #pragma unroll
    for (int j = 0; j < 8; ++j) smf1v(av, A[j], Wv[j], idx);
    f32x4 S0, S1, SM;
    if (!LAST) {
      sc3_first(S0, S1, SM, Kx[0], Kx[4], F[0]);
      sc3_next(S0, S1, SM, Kx[1], Kx[5], F[1]);
      asm volatile("" : "+v"(av));
    } else {
      mwait(av);
    }
    av = hsum4(av, av);
    u32x2 vm = {pk(av[0], av[1]), pk(av[2], av[3])};
    const unsigned slot = OFF_PART + polA + (unsigned)(s & 1) * (64u * 16384u);
    if (!LAST) {
      sc3_next(S0, S1, SM, Kx[2], Kx[6], F[2]);
      sc3_next(S0, S1, SM, Kx[3], Kx[7], F[3]);
      mwait(S0, S1, SM);
      if (l < 32) SM = (f32x4){-INFINITY, -INFINITY, -INFINITY, -INFINITY};
      const float m = max12_g(S0, S1, SM) * SM_SCALE;
      #pragma unroll
      for (int q = 0; q < 4; ++q) {
        S0[q] = __builtin_amdgcn_exp2f(__builtin_fmaf(S0[q], SM_SCALE, -m));
        S1[q] = __builtin_amdgcn_exp2f(__builtin_fmaf(S1[q], SM_SCALE, -m));
        SM[q] = __builtin_amdgcn_exp2f(__builtin_fmaf(SM[q], SM_SCALE, -m));
      }
      u32x4 Px = {pk(S0[0], S0[1]), pk(S0[2], S0[3]), pk(S1[0], S1[1]), pk(S1[2], S1[3])};
      u32x2 Pm = {pk(SM[0], SM[1]), pk(SM[2], SM[3])};
      f32x4 O, O2;
      pv2(O, O2, Vx, Px, vm, Pm);
      S0 = (S0 + S1) + SM;
      const float inv = __builtin_amdgcn_rcpf(red_sum_g((S0[0] + S0[1]) + (S0[2] + S0[3])));
      mwait(O, O2);
      O = (O + O2) * inv;
      *(u32x2*)oimg_w = (u32x2){pk(O[0], O[1]), pk(O[2], O[3])};
      f32x4 Y[8];
      #pragma unroll
      for (int t = 0; t < 8; ++t) Y[t] = zero4();
      bar();
      u32x4 Ao = *(const u32x4*)oimg_r;
      smf4v(Y[0], Y[4], Y[1], Y[5], Ao, Wo[0], Wo[4], Wo[1], Wo[5], idx);
      smf4v(Y[2], Y[6], Y[3], Y[7], Ao, Wo[2], Wo[6], Wo[3], Wo[7], idx);
      asm volatile("" : "+v"(Y[0]), "+v"(Y[4]), "+v"(Y[1]), "+v"(Y[5]));
      #pragma unroll
      for (int ab = 0; ab < 2; ++ab) {
        if (ab == 1) mwait(Y[2], Y[6], Y[3], Y[7]);
        f32x4 t0 = hsum4(Y[2 * ab], Y[2 * ab + 4]), t1 = hsum4(Y[2 * ab + 1], Y[2 * ab + 5]);
        u32x4 c = {guard(pk(t0[0], t1[0])), guard(pk(t0[1], t1[1])), guard(pk(t0[2], t1[2])), guard(pk(t0[3], t1[3]))};
        st128c(c, rw, pst, slot + ab * 16384u);
      }
    }
    __builtin_amdgcn_sched_barrier(0);
    {
      const unsigned kvmo = OFF_KVM + (kvb + s) * 4096u;
      const unsigned lb = lau(l16);
      u32x4 fw = *(const u32x4*)(qimg_r + w * 64);
      if (eh == 0 && (lb & 128u)) st128c(fw, rw, ((lb >> 1) & 0x180u) | (lb & 0x70u), kvmo + w * 512u);
      if (lb >= 512u) st64c(vm, rw, (lb >> 1) & 0xF8u, kvmo + 2048u + (4 * eh + w) * 256u);
    }
    if (LAST) {
      vm0();
      bar();
      if (tid == 64) st32c(63u, rw, 0u, OFF_FLAGS + (unsigned)(FL_R + r) * 4u);
      return;
    }
    {
      if (kvp < 63) {
        int a = (int)__builtin_amdgcn_readlane(fk, 0), b = (int)__builtin_amdgcn_readlane(fk, 1);
        kvp = a < b ? a : b;
        if (kvp < s + 1) {
          kvp = wait_flags(rw, FL_K + h, 1, s + 1, l, dead);
          int t = wait_flags(rw, FL_V + h, 1, s + 1, l, dead); kvp = t < kvp ? t : kvp;
        }
      }
      const unsigned lb = lau(l16);
      #pragma unroll
      for (int i = 0; i < 8; ++i) Kx[i] = ld128c(rw, lb + (i & 3) * 1024u, OFF_KX + (kvb + s + 1) * 8192u + (i >> 2) * 4096u);
      Vx = ld128c(rw, lb, OFF_VX + (kvb + s + 1) * 8192u + (4 * eh + w) * 1024u);
    }
    __builtin_amdgcn_sched_barrier(0);
    if (w == 0) {
      const unsigned so = slot + (unsigned)r * 16384u;
      const unsigned lb = lau(l16);
      u32x4 sel;
      #pragma unroll
      for (int q = 0; q < 4; ++q) sel[q] = ((lb >> 5) & 3u) == (unsigned)q ? ((lb & 16u) ? 0x3F800000u : 0x00003F80u) : 0u;
      asm volatile("" : "+v"(sel));
      __builtin_amdgcn_s_sleep(D1);
      f32x4 D;
      int it2 = 0;
      while (true) {
        u32x4 P[8];
        const unsigned lc = lau(l16);
        #pragma unroll
        for (int j = 0; j < 8; ++j) P[j] = ld128c(rw, lc + (j & 3) * 1024u, so + (j >> 2) * 4096u);
        __builtin_amdgcn_sched_barrier(0);
        unsigned mx = 0;
        D = zero4();
        #pragma unroll
        for (int j = 0; j < 8; ++j) mf32_acc(D, mx, P[j], sel);
        mwait(D);
        asm volatile("" ::: "memory");
        if (__builtin_amdgcn_ballot_w64(mx == 0xFFFFFFFFu) == 0ull) break;
        if (dead || ++it2 > SPIN_MAX) { dead = 1; break; }
      }
      D = hsum4(D, D);
      u32x2 v = {guard(pk(D[0], D[1])), guard(pk(D[2], D[3]))};
      if ((lb & 0x280u) == 0u) st64c(v, rw, ((lb >> 2) & 64u) | ((lb >> 1) & 56u), mo + 16384u + r * 128u);
      __builtin_amdgcn_sched_barrier(0);
      u32x4 ones = {~0u, ~0u, ~0u, ~0u};
      asm volatile("" : "+v"(ones));
      #pragma unroll
      for (int j = 0; j < 8; ++j) st128c(ones, rw, lb + (j & 3) * 1024u, so + (j >> 2) * 4096u);
    } else {
      vm0();
    }
    bar();
    if (tid == 64) st32c((unsigned)s, rw, 0u, OFF_FLAGS + (unsigned)(FL_R + r) * 4u);
    __builtin_amdgcn_s_sleep(D2);
  };
  for (int s = 0; s < 63; ++s) step(s, BoolC<false>());
  step(63, BoolC<true>());
}

template <int MODE>
DEVI void role_proj(const Params& p, int u, char* smem) {
  const int tid = threadIdx.x, l = tid & 63, w = (int)rfl(tid >> 6);
  const int n = l & 15, g = l >> 4;
  rsrc_t rw = mkrsrc(p.ws, WS_BYTES);
  rsrc_t rx = mkrsrc(p.x, 2048u * 1024u);
  int dead = 0;
  const unsigned xg = (unsigned)(8 * w + (l >> 3)) * 1024u + (unsigned)((l & 7) ^ (l >> 3)) * 16u;
  u32x4 X[8];
  #pragma unroll
  for (int i = 0; i < 8; ++i) X[i] = ld128(rx, xg + i * 128u, 0);
  u32x4 W[2][16];
  {
    const unsigned ld = (MODE == 3) ? 3584u : 8192u;
    rsrc_t rm = mkrsrc(MODE == 0 ? p.wk : MODE == 1 ? p.wv : MODE == 2 ? p.wq : p.w1, 512u * ld);
    const unsigned vo = (unsigned)(8 * g) * ld + (unsigned)u * 256u + (MODE == 1 ? n * 8u : n * 16u);
    #pragma unroll
    for (int i = 0; i < 4; ++i) {
      u32x4 a[8];
      #pragma unroll
      for (int j = 0; j < 8; ++j) {
        unsigned so = (unsigned)(32 * (4 * w + i) + j) * ld;
        if (MODE == 1) { u32x2 c = ld64(rm, vo, so), d = ld64(rm, vo + 128u, so); a[j] = (u32x4){c[0], c[1], d[0], d[1]}; }
        else a[j] = ld128(rm, vo, so);
      }
      #pragma unroll
      for (int t = 0; t < 8; ++t) {
        u32x4 o;
        #pragma unroll
        for (int q = 0; q < 4; ++q) o[q] = (t & 1) ? phi(a[2 * q][t >> 1], a[2 * q + 1][t >> 1]) : plo(a[2 * q][t >> 1], a[2 * q + 1][t >> 1]);
        *(u32x4*)(smem + (t * 16 + 4 * w + i) * 1024 + l * 16) = o;
      }
    }
    bar();
    #pragma unroll
    for (int t = 0; t < 2; ++t)
      #pragma unroll
      for (int k = 0; k < 16; ++k) W[t][k] = *(const u32x4*)(smem + ((2 * w + t) * 16 + k) * 1024 + l * 16);
    bar();
  }
  float bias[2][4];
  if (MODE == 3) {
    #pragma unroll
    for (int q = 0; q < 4; ++q)
      #pragma unroll
      for (int t = 0; t < 2; ++t) bias[t][q] = asf((unsigned)p.b1[128 * u + 8 * (4 * g + q) + 2 * w + t] << 16);
  }
  const int xr0 = (n >> 3) * 8192 + (n & 7) * 128 + ((g ^ (n & 7)) * 16);
  const int xr1 = (n >> 3) * 8192 + (n & 7) * 128 + (((4 + g) ^ (n & 7)) * 16);
  const int flag = (MODE == 0 ? FL_K : MODE == 1 ? FL_V : MODE == 2 ? FL_O : FL_H) + u;
  int kvp = -1;
  for (int s = 0; s < 64; ++s) {
    char* xb = smem + (s & 1) * 32768;
    #pragma unroll
    for (int i = 0; i < 8; ++i) *(u32x4*)(xb + (w * 8 + i) * 1024 + l * 16) = X[i];
    if (MODE != 2) asm volatile("s_waitcnt vmcnt(2)" ::: "memory");
    bar();
    if (MODE != 2 && s >= 4 && tid == 0) st32c((unsigned)(s - 2), rw, (unsigned)flag * 4u, OFF_FLAGS);
    if (s < 63) {
      #pragma unroll
      for (int i = 0; i < 8; ++i) X[i] = ld128(rx, xg + i * 128u, (unsigned)(s + 1) * 32768u);
    }
    f32x4 D[2][2];
    #pragma unroll
    for (int t = 0; t < 2; ++t) { D[t][0] = (f32x4){0.f, 0.f, 0.f, 0.f}; D[t][1] = D[t][0]; }
    #pragma unroll
    for (int k = 0; k < 16; ++k) {
      u32x4 x0 = *(const u32x4*)(xb + ((k & 1) ? xr1 : xr0) + (k >> 1) * 1024);
      u32x4 x1 = *(const u32x4*)(xb + ((k & 1) ? xr1 : xr0) + (k >> 1) * 1024 + 16384);
      #pragma unroll
      for (int t = 0; t < 2; ++t) {
        if (MODE == 1) { D[t][0] = mf32(x0, W[t][k], D[t][0]); D[t][1] = mf32(x1, W[t][k], D[t][1]); }
        else { D[t][0] = mf32(W[t][k], x0, D[t][0]); D[t][1] = mf32(W[t][k], x1, D[t][1]); }
      }
    }
    if (MODE == 0) {
      #pragma unroll
      for (int T = 0; T < 2; ++T) {
        u32x4 o = {pk(D[0][T][0], D[1][T][0]), pk(D[0][T][1], D[1][T][1]), pk(D[0][T][2], D[1][T][2]), pk(D[0][T][3], D[1][T][3])};
        st128c(o, rw, (unsigned)l * 16u, OFF_KX + (unsigned)(u * 64 + s) * 8192u + (T * 4 + w) * 1024u);
      }
    } else if (MODE == 1) {
      #pragma unroll
      for (int t = 0; t < 2; ++t) {
        u32x4 o = {pk(D[t][0][0], D[t][0][1]), pk(D[t][0][2], D[t][0][3]), pk(D[t][1][0], D[t][1][1]), pk(D[t][1][2], D[t][1][3])};
        st128c(o, rw, (unsigned)l * 16u, OFF_VX + (unsigned)(u * 64 + s) * 8192u + (2 * w + t) * 1024u);
      }
    } else if (MODE == 3) {
      #pragma unroll
      for (int T = 0; T < 2; ++T) {
        u32x4 o;
        #pragma unroll
        for (int q = 0; q < 4; ++q) {
          float a = fmaxf(D[0][T][q] + bias[0][q], 0.f), b = fmaxf(D[1][T][q] + bias[1][q], 0.f);
          o[q] = pk(a * a, b * b);
        }
        st128c(o, rw, (unsigned)((n >> 3) * 28 * 1024 + g * 128 + (n & 7) * 16),
               OFF_HF + (unsigned)((s * 4 + 2 * T) * 28 + 2 * u + (w >> 1)) * 1024u + (w & 1) * 512u);
      }
    } else {
      char* qb = smem + 65536;
      #pragma unroll
      for (int T = 0; T < 2; ++T) {
        u32x4 o = {pk(D[0][T][0], D[1][T][0]), pk(D[0][T][1], D[1][T][1]), pk(D[0][T][2], D[1][T][2]), pk(D[0][T][3], D[1][T][3])};
        *(u32x4*)(qb + (T * 4 + w) * 1024 + l * 16) = o;
      }
      if (kvp < s) {
        kvp = wait_flags(rw, FL_K + u, 1, s, l, dead);
        int t = wait_flags(rw, FL_V + u, 1, s, l, dead); kvp = t < kvp ? t : kvp;
      }
      const int TT = w & 1, eh = w >> 1;
      const unsigned hs = (unsigned)(u * 64 + s);
      u32x4 Kx[8], Vx[4], Q[4];
      #pragma unroll
      for (int i = 0; i < 8; ++i) Kx[i] = ld128c(rw, (unsigned)l * 16u + (i & 3) * 1024u, OFF_KX + hs * 8192u + (i >> 2) * 4096u);
      #pragma unroll
      for (int i = 0; i < 4; ++i) Vx[i] = ld128c(rw, (unsigned)l * 16u + i * 1024u, OFF_VX + hs * 8192u + eh * 4096u);
      bar();
      #pragma unroll
      for (int k = 0; k < 4; ++k) Q[k] = *(const u32x4*)(qb + (TT * 4 + k) * 1024 + l * 16);
      f32x4 S0 = {0.f, 0.f, 0.f, 0.f}, S1 = S0, SM = S0;
      #pragma unroll
      for (int k = 0; k < 4; ++k) { S0 = mf32(Kx[k], Q[k], S0); S1 = mf32(Kx[4 + k], Q[k], S1); }
      #pragma unroll
      for (int q = 0; q < 4; ++q) {
        if (4 * g + q > 16 * TT + n) S0[q] = -INFINITY;
        if (16 + 4 * g + q > 16 * TT + n) S1[q] = -INFINITY;
      }
      wait_flags(rw, FL_R + 2 * u, 2, s, l, dead);
      u32x4 Km[4]; u32x2 Vm[4];
      #pragma unroll
      for (int k = 0; k < 4; ++k) Km[k] = ld128c(rw, (unsigned)(g * 128 + (n & 7) * 16 + k * 512), OFF_KVM + hs * 4096u);
      #pragma unroll
      for (int i = 0; i < 4; ++i) Vm[i] = ld64c(rw, (unsigned)((l & 31) * 8 + i * 256), OFF_KVM + hs * 4096u + 2048u + eh * 1024u);
      #pragma unroll
      for (int k = 0; k < 4; ++k) SM = mf32(Km[k], Q[k], SM);
      if (l < 32) SM = (f32x4){-INFINITY, -INFINITY, -INFINITY, -INFINITY};
      float m = fmaxf(fmaxf(fmaxf(S0[0], S0[1]), fmaxf(S0[2], S0[3])), fmaxf(fmaxf(S1[0], S1[1]), fmaxf(S1[2], S1[3])));
      m = fmaxf(m, fmaxf(fmaxf(SM[0], SM[1]), fmaxf(SM[2], SM[3])));
      m = red_max_g(m) * SM_SCALE;
      #pragma unroll
      for (int q = 0; q < 4; ++q) {
        S0[q] = __builtin_amdgcn_exp2f(__builtin_fmaf(S0[q], SM_SCALE, -m));
        S1[q] = __builtin_amdgcn_exp2f(__builtin_fmaf(S1[q], SM_SCALE, -m));
        SM[q] = __builtin_amdgcn_exp2f(__builtin_fmaf(SM[q], SM_SCALE, -m));
      }
      u32x4 Px = {pk(S0[0], S0[1]), pk(S0[2], S0[3]), pk(S1[0], S1[1]), pk(S1[2], S1[3])};
      u32x2 Pm = {pk(SM[0], SM[1]), pk(SM[2], SM[3])};
      f32x4 O[4];
      #pragma unroll
      for (int i = 0; i < 4; ++i) { O[i] = mf32(Vx[i], Px, (f32x4){0.f, 0.f, 0.f, 0.f}); O[i] = mf16(Vm[i], Pm, O[i]); }
      float sum = ((S0[0] + S0[1]) + (S0[2] + S0[3])) + ((S1[0] + S1[1]) + (S1[2] + S1[3])) + ((SM[0] + SM[1]) + (SM[2] + SM[3]));
      float inv = __builtin_amdgcn_rcpf(red_sum_g(sum));
      #pragma unroll
      for (int q = 0; q < 2; ++q) {
        u32x4 o = {pk(O[2 * q][0] * inv, O[2 * q][1] * inv), pk(O[2 * q][2] * inv, O[2 * q][3] * inv),
                   pk(O[2 * q + 1][0] * inv, O[2 * q + 1][1] * inv), pk(O[2 * q + 1][2] * inv, O[2 * q + 1][3] * inv)};
        st128c(o, rw, (unsigned)((n >> 3) * 65536 + q * 512 + g * 128 + (n & 7) * 16),
               OFF_OF + (unsigned)((s * 4 + 2 * TT) * 64 + 2 * u + eh) * 1024u);
      }
    }
    if (MODE == 2 || s < 2 || s == 63) {
      vm0();
      bar();
      if (tid == 0) st32c((unsigned)s, rw, (unsigned)flag * 4u, OFF_FLAGS);
    }
  }
}

DEVI void role_gate(const Params& p, int u) {
  const int tid = threadIdx.x, l = tid & 63, w = (int)rfl(tid >> 6);
  rsrc_t rw = mkrsrc(p.ws, WS_BYTES);
  rsrc_t rx = mkrsrc(p.x, 2048u * 1024u);
  u32x4 gw = *(const u32x4*)(p.gw + l * 8);
  float mine = 0.f;
  for (int b = 0; b < 4; ++b) {
    u32x4 X[8];
    #pragma unroll
    for (int i = 0; i < 8; ++i) X[i] = ld128(rx, (unsigned)l * 16u, (unsigned)(u * 128 + w * 32 + b * 8 + i) * 1024u);
    #pragma unroll
    for (int i = 0; i < 8; ++i) {
      float a = 0.f;
      #pragma unroll
      for (int q = 0; q < 4; ++q) {
        a = __builtin_fmaf(asf(X[i][q] << 16), asf(gw[q] << 16), a);
        a = __builtin_fmaf(asf(X[i][q] & 0xFFFF0000u), asf(gw[q] & 0xFFFF0000u), a);
      }
      #pragma unroll
      for (int o = 1; o < 64; o <<= 1) a += __shfl_xor(a, o);
      if (l == b * 8 + i) mine = a;
    }
  }
  float gv = 1.f / (1.f + __expf(-mine));
  if (l < 32) st32c(asu(gv), rw, (unsigned)(u * 128 + w * 32 + l) * 4u, OFF_G);
  vm0();
  bar();
  if (tid == 0) st32c(0u, rw, (unsigned)(FL_G + u) * 4u, OFF_FLAGS);
}

DEVI void role_final(const Params& p, int G, int C, char* smem) {
  const int tid = threadIdx.x, l = tid & 63, w = (int)rfl(tid >> 6);
  const int n = l & 15, gb = l >> 4;
  rsrc_t rw = mkrsrc(p.ws, WS_BYTES);
  int dead = 0;
  u32x8 WA[16][2], WB[7][2];
  {
    rsrc_t r2 = mkrsrc(p.w2, 1792u * 1024u);
    const unsigned vo = (unsigned)(64 * (gb >> 1) + 16 * (gb & 1)) * 1024u + (32 * C + 2 * n) * 2u;
    #pragma unroll
    for (int i = 0; i < 7; ++i) {
      const int c = 7 * w + i;
      const unsigned cb = (unsigned)(128 * (c >> 1) + 4 * (c & 1)) * 1024u;
      unsigned a[8], b[8];
      #pragma unroll
      for (int m = 0; m < 8; ++m) {
        unsigned ro = cb + (unsigned)(32 * (m & 1) + 8 * ((m >> 1) & 1) + 2 * (m >> 2)) * 1024u;
        a[m] = ld32(r2, vo, ro); b[m] = ld32(r2, vo + 1024u, ro);
      }
      #pragma unroll
      for (int m = 0; m < 8; ++m) { WB[i][0][m] = plo(a[m], b[m]); WB[i][1][m] = phi(a[m], b[m]); }
      if (i & 1) __builtin_amdgcn_sched_barrier(0);
    }
    rsrc_t ro_ = mkrsrc(p.wo, 4096u * 1024u);
    const unsigned vo2 = (unsigned)(32 * (gb >> 1) + (gb & 1)) * 1024u + (32 * C + 2 * n) * 2u;
    #pragma unroll
    for (int i = 0; i < 16; ++i) {
      const unsigned cb = (unsigned)(64 * (16 * w + i)) * 1024u;
      unsigned a[8], b[8];
      #pragma unroll
      for (int m = 0; m < 8; ++m) {
        unsigned ro = cb + (unsigned)(16 * (m & 1) + 8 * ((m >> 1) & 1) + 2 * (m >> 2)) * 1024u;
        a[m] = ld32(ro_, vo2, ro); b[m] = ld32(ro_, vo2, ro + 4096u);
      }
      #pragma unroll
      for (int m = 0; m < 8; ++m) { WA[i][0][m] = plo(a[m], b[m]); WA[i][1][m] = phi(a[m], b[m]); }
      pin_a(WA[i][0]); pin_a(WA[i][1]);
      if (i & 1) __builtin_amdgcn_sched_barrier(0);
    }
  }
  const int idx = (l & 8) ? 0xEEEE : 0x4444;
  const unsigned l16 = (unsigned)l * 16u;
  const int oi = (tid >> 4) & 7, cp = tid & 15;
  const unsigned b2p = *(const unsigned*)(p.b2 + 32 * C + 2 * cp);
  wait_flags(rw, FL_G, 16, 0, l, dead);
  int hp = -1;
  for (int s = 0; s < 64; ++s) {
    const unsigned unit = (unsigned)(s * 4 + G);
    if (hp < s) hp = wait_flags(rw, FL_H, 14, s, l, dead);
    f32x4 B0 = {0.f, 0.f, 0.f, 0.f}, B1 = B0, A0 = B0, A1 = B0;
    {
      u32x4 Hh[7];
      #pragma unroll
      for (int i = 0; i < 7; ++i) Hh[i] = ld128c(rw, l16 + (i & 3) * 1024u, OFF_HF + (unit * 28u + 7 * w) * 1024u + (i >> 2) * 4096u);
      #pragma unroll
      for (int i = 0; i < 7; ++i) smf2v(B0, B1, Hh[i], WB[i][0], WB[i][1], idx);
      mwait(B0, B1);
    }
    float gv = asf(ld32c(rw, (unsigned)oi * 4u, OFF_G + unit * 32u));
    char* red = smem + (s & 1) * 8192;
    char* rwp = red + w * 2048 + (16 * (gb >> 1) + n) * 32 + 16 * (gb & 1);
    *(f32x4*)(rwp + 1024) = hsum4(B0, B1);
    wait_flags(rw, FL_O + 8 * w, 8, s, l, dead);
    {
      u32x4 Oo[16];
      #pragma unroll
      for (int i = 0; i < 16; ++i) Oo[i] = ld128c(rw, l16 + (i & 3) * 1024u, OFF_OF + (unit * 64u + 16 * w) * 1024u + (i >> 2) * 4096u);
      #pragma unroll
      for (int i = 0; i < 16; ++i) smf2a(A0, A1, Oo[i], WA[i][0], WA[i][1], idx);
      mwait(A0, A1);
    }
    *(f32x4*)rwp = hsum4(A0, A1);
    bar();
    if (tid < 128) {
      float a0 = 0.f, a1 = 0.f, b0 = asf(b2p << 16), b1 = asf(b2p & 0xFFFF0000u);
      #pragma unroll
      for (int q = 0; q < 4; ++q) {
        const float* rp = (const float*)(red + q * 2048 + cp * 32 + oi * 4);
        a0 += rp[0]; a1 += rp[128]; b0 += rp[256]; b1 += rp[384];
      }
      *(unsigned*)(p.out + (unit * 8u + oi) * 512u + 32 * C + 2 * cp) = pk(gv * a0 + (1.f - gv) * b0, gv * a1 + (1.f - gv) * b1);
    }
  }
}

__global__ void __launch_bounds__(256, 1) gated_mix_kernel(Params p) {
  extern __shared__ __attribute__((aligned(16))) char smem[];
  const int b = blockIdx.x, x = b & 7, i = b >> 3;
  if (x < 4) {
    if (i < 16) role_rec(p, x * 16 + i, smem);
    else if (i < 24) role_proj<0>(p, x * 8 + (i - 16), smem);
    else role_proj<1>(p, x * 8 + (i - 24), smem);
  } else {
    const int y = x - 4;
    if (i < 8) role_proj<2>(p, y * 8 + i, smem);
    else if (i < 12) role_gate(p, y * 4 + (i - 8));
    else if (i < 16) { int u = y * 4 + (i - 12); if (u < 14) role_proj<3>(p, u, smem); }
    else role_final(p, y, i - 16, smem);
  }
}

extern "C" void kernel_launch(void* const* d_in, const int* in_sizes, int n_in,
                              void* d_out, int out_size, void* d_ws, size_t ws_size,
                              hipStream_t stream) {
  Params p;
  p.x = (const uint16_t*)d_in[0]; p.mem0 = (const uint16_t*)d_in[1];
  p.wq = (const uint16_t*)d_in[2]; p.wk = (const uint16_t*)d_in[3]; p.wv = (const uint16_t*)d_in[4]; p.wo = (const uint16_t*)d_in[5];
  p.w1 = (const uint16_t*)d_in[8]; p.b1 = (const uint16_t*)d_in[9]; p.w2 = (const uint16_t*)d_in[10]; p.b2 = (const uint16_t*)d_in[11];
  p.gw = (const uint16_t*)d_in[12];
  p.out = (uint16_t*)d_out;
  p.ws = (char*)(((uintptr_t)d_ws + (2u << 20) - 1) & ~(uintptr_t)((2u << 20) - 1));
  (void)hipFuncSetAttribute((const void*)gated_mix_kernel, hipFuncAttributeMaxDynamicSharedMemorySize, 131072);
  (void)hipMemsetAsync(p.ws, 0xFF, 32768, stream);
  gated_mix_kernel<<<256, 256, 131072, stream>>>(p);
}
```

```cpp
#include <hip/hip_runtime.h>
#include <hip/hip_bf16.h>
#include <stdint.h>

typedef __attribute__((ext_vector_type(2))) unsigned u32x2;
typedef __attribute__((ext_vector_type(4))) unsigned u32x4;
typedef __attribute__((ext_vector_type(8))) unsigned u32x8;
typedef __attribute__((ext_vector_type(2))) float f32x2;
typedef __attribute__((ext_vector_type(4))) float f32x4;
typedef __attribute__((ext_vector_type(2))) __bf16 bf16x2;
typedef __attribute__((ext_vector_type(8))) __bf16 bf16x8;
typedef __attribute__((ext_vector_type(16))) __bf16 bf16x16;
typedef __attribute__((ext_vector_type(4))) short s16x4;
typedef __amdgpu_buffer_rsrc_t rsrc_t;

#define D1 30
#define XM0 85

#define PERC 4870.f
#define XMC 1255.f
#define RGD 4
#define FN 3
#define EUP 5
#define EMAX 12
#define TOL 6
#define BJ 5
#define RJ 7
#define TOL1 10
#define TOLM 10
#define PRT 300
#define BSL 12
#define QK2 8
#define QSL 4
#define RIM 58
#define RG0 20
#define EXT0 6
#define Y0X 35u
#define FS0 5
#define FUP 2
#define FMAX 8
#define RLX 0u
#define P2D 30.f
#define ECAS 1
#define DG 3
#define PFLIP 0u
#define DF 3
#define DQ 2
#define PSL 2
#define SC1 16
#define DEVI static __device__ __forceinline__

#define OFF_FLAGS ((2u << 20) - 32768u)
#define OFF_ACK   ((2u << 20) - 65536u)
#define OFF_RF    ((2u << 20) - 98304u)
#define OFF_RL    ((2u << 20) - 81920u)
#define OFF_MEM   (2u << 20)
#define OFF_P2    (3u << 20)
#define OFF_PART  (4u << 20)
#define OFF_KX    (6u << 20)
#define OFF_VX    (22u << 20)
#define OFF_KVM   (38u << 20)
#define OFF_OF    (46u << 20)
#define OFF_HF    (62u << 20)
#define OFF_G     (69u << 20)
#define WS_BYTES  (72u << 20)
#define TSK(i)
#define TSR(i)
#define TSV(i, v)
#define TSDECL
#define TSOUT(slot, s)
#define FL_K 0
#define FL_V 32
#define FL_O 128
#define FL_H 160
#define FL_G 192
#define FL_W 254
#define FL_P 255
#define SPIN_MAX (1 << 17)

DEVI rsrc_t mkrsrc(const void* p, unsigned bytes) { return __builtin_amdgcn_make_buffer_rsrc((void*)p, 0, bytes, 0x00020000); }
DEVI u32x4 ld128(rsrc_t r, unsigned v, unsigned s) { return __builtin_amdgcn_raw_buffer_load_b128(r, v, s, 0); }
DEVI u32x2 ld64(rsrc_t r, unsigned v, unsigned s) { return __builtin_amdgcn_raw_buffer_load_b64(r, v, s, 0); }
DEVI unsigned ld32(rsrc_t r, unsigned v, unsigned s) { return __builtin_amdgcn_raw_buffer_load_b32(r, v, s, 0); }
DEVI u32x4 ld128c(rsrc_t r, unsigned v, unsigned s) { return __builtin_amdgcn_raw_buffer_load_b128(r, v, s, SC1); }
DEVI u32x2 ld64c(rsrc_t r, unsigned v, unsigned s) { return __builtin_amdgcn_raw_buffer_load_b64(r, v, s, SC1); }
DEVI unsigned ld32c(rsrc_t r, unsigned v, unsigned s) { return __builtin_amdgcn_raw_buffer_load_b32(r, v, s, SC1); }
DEVI void st128c(u32x4 d, rsrc_t r, unsigned v, unsigned s) { __builtin_amdgcn_raw_buffer_store_b128(d, r, v, s, SC1); }
DEVI void st64c(u32x2 d, rsrc_t r, unsigned v, unsigned s) { __builtin_amdgcn_raw_buffer_store_b64(d, r, v, s, SC1); }
DEVI void st32c(unsigned d, rsrc_t r, unsigned v, unsigned s) { __builtin_amdgcn_raw_buffer_store_b32(d, r, v, s, SC1); }
DEVI unsigned pk(float a, float b) { f32x2 v = {a, b}; return __builtin_bit_cast(unsigned, __builtin_convertvector(v, bf16x2)); }
DEVI unsigned plo(unsigned a, unsigned b) { return __builtin_amdgcn_perm(b, a, 0x05040100u); }
DEVI unsigned phi(unsigned a, unsigned b) { return __builtin_amdgcn_perm(b, a, 0x07060302u); }
DEVI float asf(unsigned x) { return __builtin_bit_cast(float, x); }
DEVI unsigned asu(float x) { return __builtin_bit_cast(unsigned, x); }
DEVI f32x4 smf(u32x4 a, u32x8 b, f32x4 c, int idx) {
  return __builtin_amdgcn_smfmac_f32_16x16x64_bf16(__builtin_bit_cast(bf16x8, a), __builtin_bit_cast(bf16x16, b), c, idx, 0, 0);
}
DEVI f32x4 mf32(u32x4 a, u32x4 b, f32x4 c) {
  return __builtin_amdgcn_mfma_f32_16x16x32_bf16(__builtin_bit_cast(bf16x8, a), __builtin_bit_cast(bf16x8, b), c, 0, 0, 0);
}
DEVI f32x4 mf16(u32x2 a, u32x2 b, f32x4 c) {
  return __builtin_amdgcn_mfma_f32_16x16x16bf16_1k(__builtin_bit_cast(s16x4, a), __builtin_bit_cast(s16x4, b), c, 0, 0, 0);
}
#define SMF "v_smfmac_f32_16x16x64_bf16 "
#define MF32 "v_mfma_f32_16x16x32_bf16 "
DEVI void smf4a(f32x4& c0, f32x4& c1, f32x4& c2, f32x4& c3, unsigned& mx, u32x4 a, const u32x8& b0, const u32x8& b1, const u32x8& b2, const u32x8& b3, int idx) {
  asm volatile(SMF "%0, %5, %6, %10\n\tv_max3_u32 %4, %4, %11, %12\n\t" SMF "%1, %5, %7, %10\n\tv_max3_u32 %4, %4, %13, %14\n\t"
               SMF "%2, %5, %8, %10\n\t" SMF "%3, %5, %9, %10"
               : "+v"(c0), "+v"(c1), "+v"(c2), "+v"(c3), "+v"(mx)
               : "v"(a), "a"(b0), "a"(b1), "a"(b2), "a"(b3), "v"(idx), "v"(a[0]), "v"(a[1]), "v"(a[2]), "v"(a[3]));
}
DEVI void smfchain(f32x4& c, const u32x4* a, const u32x8* b, int idx) {
  asm volatile(SMF "%0, %1, %9, %17\n\t" SMF "%0, %2, %10, %17\n\t" SMF "%0, %3, %11, %17\n\t" SMF "%0, %4, %12, %17\n\t"
               SMF "%0, %5, %13, %17\n\t" SMF "%0, %6, %14, %17\n\t" SMF "%0, %7, %15, %17\n\t" SMF "%0, %8, %16, %17"
               : "+v"(c) : "v"(a[0]), "v"(a[1]), "v"(a[2]), "v"(a[3]), "v"(a[4]), "v"(a[5]), "v"(a[6]), "v"(a[7]),
                 "v"(b[0]), "v"(b[1]), "v"(b[2]), "v"(b[3]), "v"(b[4]), "v"(b[5]), "v"(b[6]), "v"(b[7]), "v"(idx));
}
DEVI void setm0(unsigned v) { asm volatile("s_mov_b32 m0, %0" :: "s"(v) : "memory"); }
template <int OFS> DEVI void ldlds(unsigned vo, u32x4 rs, unsigned so) {
  asm volatile("buffer_load_dwordx4 %0, %1, %2 offen offset:%3 sc1 lds" :: "v"(vo), "s"(rs), "s"(so), "n"(OFS) : "memory");
}
DEVI void smf1(f32x4& c, u32x4 a, const u32x8& b, int idx) { asm volatile(SMF "%0, %1, %2, %3" : "+v"(c) : "v"(a), "v"(b), "v"(idx)); }
DEVI void mf0(f32x4& c, u32x4 a, u32x4 b) { asm volatile(MF32 "%0, %1, %2, 0" : "=&v"(c) : "v"(a), "v"(b)); }
DEVI void mfc(f32x4& c, u32x4 a, u32x4 b, f32x4 i) { asm volatile(MF32 "%0, %1, %2, %3" : "=&v"(c) : "v"(a), "v"(b), "v"(i)); }
DEVI void mfa(f32x4& c, u32x4 a, u32x4 b) { asm volatile(MF32 "%0, %1, %2, %0" : "+v"(c) : "v"(a), "v"(b)); }
DEVI void swp(float& a, float& b) { asm volatile("v_permlane32_swap_b32 %0, %1" : "+v"(a), "+v"(b)); }
DEVI float vmov(float a) { float d; asm volatile("v_mov_b32 %0, %1" : "=v"(d) : "v"(a)); return d; }
DEVI float vadd(float a, float b) { float d; asm volatile("v_add_f32 %0, %1, %2" : "=v"(d) : "v"(a), "v"(b)); return d; }
DEVI unsigned vcvt(float a, float b) { unsigned d; asm volatile("v_cvt_pk_bf16_f32 %0, %1, %2" : "=v"(d) : "v"(a), "v"(b)); return d; }
DEVI f32x2 zero2() { f32x2 a; asm volatile("v_mov_b64 %0, 0" : "=v"(a)); return a; }
DEVI f32x4 cat(f32x2 a, f32x2 b) { return (f32x4){a[0], a[1], b[0], b[1]}; }
#define SB() __builtin_amdgcn_sched_barrier(0)
#define NOP_(n) asm volatile("s_nop " #n)
#define NOP(n) NOP_(n)
DEVI void smf2a(f32x4& c0, f32x4& c1, u32x4 a, const u32x8& b0, const u32x8& b1, int idx) {
  asm volatile("s_nop 1\n\t" SMF "%0, %2, %3, %5\n\t" SMF "%1, %2, %4, %5" : "+v"(c0), "+v"(c1) : "v"(a), "a"(b0), "a"(b1), "v"(idx));
}
DEVI void smf2v(f32x4& c0, f32x4& c1, u32x4 a, const u32x8& b0, const u32x8& b1, int idx) {
  asm volatile("s_nop 1\n\t" SMF "%0, %2, %3, %5\n\t" SMF "%1, %2, %4, %5" : "+v"(c0), "+v"(c1) : "v"(a), "v"(b0), "v"(b1), "v"(idx));
}
DEVI void mf32_acc(f32x4& c, unsigned& mx, u32x4 a, u32x4 b) {
  asm volatile(MF32 "%0, %2, %3, %0\n\tv_max3_u32 %1, %1, %4, %5\n\tv_max3_u32 %1, %1, %6, %7"
               : "+v"(c), "+v"(mx) : "v"(a), "v"(b), "v"(a[0]), "v"(a[1]), "v"(a[2]), "v"(a[3]));
}
DEVI void mwait(f32x4& a) { asm volatile("s_nop 7" : "+v"(a)); }
DEVI void mwait(f32x4& a, f32x4& b) { asm volatile("s_nop 7" : "+v"(a), "+v"(b)); }
DEVI void mwait(f32x4& a, f32x4& b, f32x4& c) { asm volatile("s_nop 7" : "+v"(a), "+v"(b), "+v"(c)); }
DEVI void mwait(f32x4& a, f32x4& b, f32x4& c, f32x4& d) { asm volatile("s_nop 7" : "+v"(a), "+v"(b), "+v"(c), "+v"(d)); }
DEVI void pin_a(u32x8& x) { asm volatile("" : "+a"(x)); }
DEVI void pin_v(u32x8& x) { asm volatile("" : "+v"(x)); }
DEVI float hsum(float a, float b) { u32x2 r = __builtin_amdgcn_permlane32_swap(asu(a), asu(b), false, false); return asf(r[0]) + asf(r[1]); }
DEVI f32x4 hsum4(f32x4 a, f32x4 b) {
  #pragma unroll
  for (int q = 0; q < 4; ++q) { u32x2 r = __builtin_amdgcn_permlane32_swap(asu(a[q]), asu(b[q]), false, false); a[q] = asf(r[0]); b[q] = asf(r[1]); }
  return a + b;
}
DEVI float vmax3(float a, float b, float c) { float d; asm("v_max3_f32 %0, %1, %2, %3" : "=v"(d) : "v"(a), "v"(b), "v"(c)); return d; }
DEVI float negmax12(f32x4 a, f32x4 b, f32x4 c) {
  float m, t;
  asm volatile("v_max3_f32 %0, %2, %3, %4\n\tv_max3_f32 %1, %5, %6, %7\n\tv_max3_f32 %0, %0, %8, %9\n\tv_max3_f32 %1, %1, %10, %11\n\t"
      "v_max3_f32 %0, %0, %12, %13\n\tv_max_f32 %0, %0, %1\n\tv_mov_b32 %1, %0\n\ts_nop 1\n\tv_permlane32_swap_b32 %0, %1\n\tv_max_f32 %0, %0, %1\n\t"
      "v_mov_b32 %1, %0\n\ts_nop 1\n\tv_permlane16_swap_b32 %0, %1\n\tv_max_f32 %0, %0, %1\n\tv_mul_f32 %0, 0xbe0293ee, %0"
      : "=&v"(m), "=&v"(t) : "v"(a[0]), "v"(a[1]), "v"(a[2]), "v"(a[3]), "v"(b[0]), "v"(b[1]), "v"(b[2]), "v"(b[3]), "v"(c[0]), "v"(c[1]), "v"(c[2]), "v"(c[3]));
  return m;
}
DEVI void pvblock(f32x4& L, f32x4& O, f32x4& z0, f32x4& z1, u32x4 one, u32x4 px, u32x4 pm, u32x4 vx, u32x4 vm) {
  f32x2 a, b, c, d;
  asm volatile("s_nop 1\n\t" MF32 "%0, %6, %7, 0\n\t" MF32 "%0, %6, %8, %0\n\tv_mov_b64 %2, 0\n\tv_mov_b64 %3, 0\n\t"
               MF32 "%1, %9, %7, 0\n\tv_mov_b64 %4, 0\n\tv_mov_b64 %5, 0\n\t" MF32 "%1, %10, %8, %1"
               : "=&v"(L), "=&v"(O), "=&v"(a), "=&v"(b), "=&v"(c), "=&v"(d) : "v"(one), "v"(px), "v"(pm), "v"(vx), "v"(vm));
  z0 = cat(a, b); z1 = cat(c, d);
}
DEVI unsigned lau(unsigned x) { asm volatile("" : "+v"(x)); return x; }
DEVI f32x4 zero4() {
  f32x2 a, b;
  asm volatile("v_mov_b64 %0, 0\n\tv_mov_b64 %1, 0" : "=v"(a), "=v"(b));
  return (f32x4){a[0], a[1], b[0], b[1]};
}
DEVI float red_max_g(float v) {
  u32x2 r = __builtin_amdgcn_permlane32_swap(asu(v), asu(v), false, false); v = fmaxf(asf(r[0]), asf(r[1]));
  r = __builtin_amdgcn_permlane16_swap(asu(v), asu(v), false, false); return fmaxf(asf(r[0]), asf(r[1]));
}
DEVI float red_sum_g(float v) {
  u32x2 r = __builtin_amdgcn_permlane32_swap(asu(v), asu(v), false, false); v = asf(r[0]) + asf(r[1]);
  r = __builtin_amdgcn_permlane16_swap(asu(v), asu(v), false, false); return asf(r[0]) + asf(r[1]);
}
DEVI unsigned umax3(unsigned a, unsigned b, unsigned c) { unsigned m = a > b ? a : b; return m > c ? m : c; }
DEVI unsigned guard(unsigned x) { return x < 0xFFFFFFFEu ? x : 0xFFFFFFFEu; }
DEVI void bar() { asm volatile("s_waitcnt lgkmcnt(0)\n\ts_barrier" ::: "memory"); }
DEVI void vm0() { asm volatile("s_waitcnt vmcnt(0)" ::: "memory"); }
DEVI unsigned rfl(unsigned x) { return __builtin_amdgcn_readfirstlane(x); }
DEVI int wait_flags(rsrc_t rw, int first, int n, int need, int lane, int& dead) {
  unsigned off = (unsigned)(first + (lane < n ? lane : 0)) * 128u;
  int it = 0;
  while (true) {
    int v = (int)ld32c(rw, off, OFF_FLAGS);
    asm volatile("" ::: "memory");
    if (__builtin_amdgcn_ballot_w64(v < need) == 0ull) {
      int m = v;
      #pragma unroll
      for (int o = 1; o < 32; o <<= 1) { int t = __shfl_xor(m, o); m = t < m ? t : m; }
      return (int)rfl((unsigned)m);
    }
    if (dead || ++it > SPIN_MAX) { dead = 1; return need; }
    __builtin_amdgcn_s_sleep(PSL);
  }
}

DEVI unsigned rtc() { return (unsigned)__builtin_amdgcn_s_memrealtime(); }
DEVI unsigned grid_wait(unsigned t, unsigned rg, int tol, unsigned now) {
  while ((int)(now - t) > tol) { t += rg; asm volatile("" : "+s"(t)); }
  while ((int)(now - t) < -3) now = rtc();
  const int rem = (int)(t - now);
  if (rem > 0) { NOP(FN); if (rem > 1) { NOP(FN); if (rem > 2) NOP(FN); } }
  return t;
}
DEVI int lane_id() { int l; asm volatile("v_mbcnt_lo_u32_b32 %0, -1, 0\n\tv_mbcnt_hi_u32_b32 %0, -1, %0" : "=v"(l)); __builtin_assume(l >= 0 && l < 64); return l; }
DEVI unsigned ldflag(rsrc_t rw, int f) {
  asm volatile("" ::: "memory");
  const unsigned v = ld32c(rw, 0u, OFF_FLAGS + (unsigned)f * 128u);
  asm volatile("" ::: "memory");
  return v;
}
DEVI unsigned ldbeat(rsrc_t rw) { return ldflag(rw, FL_P); }
DEVI unsigned beat_wait(rsrc_t rw, unsigned g0, unsigned g1, unsigned c, int& dead) {
  const unsigned e = rfl(g0);
  unsigned v = rfl(g1);
  int it = 0;
  while (v == e) {
    if (++it > SPIN_MAX) { dead = 1; return c; }
    __builtin_amdgcn_s_sleep(PSL);
    v = rfl(ldbeat(rw));
  }
  return v;
}
DEVI unsigned pw_wait(rsrc_t rw, unsigned pw, int& dead) {
  pw = rfl(pw);
  for (int it = 0; pw > 3u; ++it) {
    if (it > SPIN_MAX) { dead = 1; pw = 0u; break; }
    __builtin_amdgcn_s_sleep(PSL);
    pw = rfl(ldflag(rw, FL_W));
  }
  return rfl(pw);
}
DEVI void beat_ack(rsrc_t rw, int tid) { if (tid == 0) st32c(0u, rw, 0u, OFF_ACK + blockIdx.x * 128u); }
template <bool B> struct BoolC { static constexpr bool value = B; };
struct Params {
  const uint16_t* x; const uint16_t* mem0; const uint16_t* wq; const uint16_t* wk; const uint16_t* wv; const uint16_t* wo;
  const uint16_t* w1; const uint16_t* b1; const uint16_t* w2; const uint16_t* b2; const uint16_t* gw;
  uint16_t* out; char* ws;
};

#define ABUF 24576u
DEVI unsigned mem_step(int s) { return OFF_MEM + (unsigned)(s >> 1) * 32768u + (unsigned)(s & 1) * 128u; }
#define SM_SCALE 0.12751743f

DEVI unsigned role_rec(const Params& p, int r, bool again, unsigned gs, unsigned c, int wv, char* smem) {
  const int l = lane_id(), w = wv, tid = w * 64 + l;
  const int h = r >> 1, eh = r & 1;
  const int n = l & 15, gb = l >> 4;
  rsrc_t rw = mkrsrc(p.ws, WS_BYTES);
  const unsigned g0 = again ? gs : ldbeat(rw);
  const unsigned mpc0 = (unsigned)((r & 15) * 256 + ((r >> 4) & 1) * 4096 + (r >> 5) * 16384);
  unsigned pw, kf0;
  int dead = 0;
  TSDECL;
  TSR(0);
  const unsigned kvb = (unsigned)h * 64u;
  int kvp;
  const u32x4 rsw = {(unsigned)(uintptr_t)p.ws, (unsigned)((uintptr_t)p.ws >> 32) & 0xFFFFu, WS_BYTES, 0x00020000u};
  unsigned pfb[3];
  #pragma unroll
  for (int i = 0; i < 3; ++i) { const int j = 3 * w + i; pfb[i] = (j < 8 ? OFF_KX + j * 1024u : OFF_VX + eh * 4096u + (j - 8) * 1024u) + (kvb + 1u) * 8192u - i * 1024u; }
  const unsigned pfm = 8192u + 3072u * w;
  u32x8 Wqk[4][8], Wv[8], Wo[8];
  const unsigned rowl = 4 * (gb >> 1) + (gb & 1);
  {
    rsrc_t rv = mkrsrc(p.wv, 512u * 4096u * 2u);
    rsrc_t rq = mkrsrc(w < 2 ? p.wq : p.wk, 512u * 4096u * 2u);
    rsrc_t ro_ = mkrsrc(p.wo, 4096u * 512u * 2u);
    const unsigned vov = rowl * 8192u + (h * 128 + 64 * eh + 4 * n) * 2u;
    const unsigned voq = rowl * 8192u + (h * 128 + 8 * n) * 2u;
    const unsigned voo = (unsigned)(h * 128 + 64 * eh + 16 * (gb & 1) + (gb >> 1)) * 1024u + (128 * w + 8 * n) * 2u;
    const int wh = w & 1;
    u32x2 va[2][8], vb[2][8];
    u32x4 qa[4][8], qb[4][8];
    #pragma unroll
    for (int jj = 0; jj < 2; ++jj)
      #pragma unroll
      for (int m = 0; m < 8; ++m) {
        unsigned ro = (unsigned)(64 * (4 * wh + jj) + 32 * (m >> 2) + 16 * ((m >> 1) & 1) + 2 * (m & 1)) * 8192u;
        qa[jj][m] = ld128(rq, voq, ro); qb[jj][m] = ld128(rq, voq, ro + 8u * 8192u);
      }
    #pragma unroll
    for (int jj = 0; jj < 2; ++jj)
      #pragma unroll
      for (int m = 0; m < 8; ++m) {
        unsigned ro = (unsigned)(64 * (2 * w + jj) + 32 * (m >> 2) + 16 * ((m >> 1) & 1) + 2 * (m & 1)) * 8192u;
        va[jj][m] = ld64(rv, vov, ro); vb[jj][m] = ld64(rv, vov, ro + 8u * 8192u);
      }
    const unsigned g1 = ldbeat(rw);
    __builtin_amdgcn_sched_barrier(0);
    char* const xw = smem + 73728 + w * 16384 + l * 16;
    auto split = [&](int jj) {
      #pragma unroll
      for (int t = 0; t < 4; ++t) {
        u32x8 own, oth;
        #pragma unroll
        for (int m = 0; m < 8; ++m) {
          unsigned lo = (t & 1) ? phi(qa[jj][m][t >> 1], qb[jj][m][t >> 1]) : plo(qa[jj][m][t >> 1], qb[jj][m][t >> 1]);
          unsigned hi2 = (t & 1) ? phi(qa[jj][m][2 + (t >> 1)], qb[jj][m][2 + (t >> 1)]) : plo(qa[jj][m][2 + (t >> 1)], qb[jj][m][2 + (t >> 1)]);
          own[m] = wh ? hi2 : lo; oth[m] = wh ? lo : hi2;
        }
        pin_a(own);
        Wqk[t][jj] = own;
        *(u32x4*)(xw + ((t * 2 + (jj & 1)) * 2) * 1024) = (u32x4){oth[0], oth[1], oth[2], oth[3]};
        *(u32x4*)(xw + ((t * 2 + (jj & 1)) * 2 + 1) * 1024) = (u32x4){oth[4], oth[5], oth[6], oth[7]};
      }
    };
    auto fetch = [&](int jj) {
      const char* xr = smem + 73728 + (w ^ 1) * 16384 + l * 16;
      #pragma unroll
      for (int t = 0; t < 4; ++t) {
        u32x4 a = *(const u32x4*)(xr + ((t * 2 + (jj & 1)) * 2) * 1024), b = *(const u32x4*)(xr + ((t * 2 + (jj & 1)) * 2 + 1) * 1024);
        u32x8 v = {a[0], a[1], a[2], a[3], b[0], b[1], b[2], b[3]};
        pin_a(v);
        Wqk[t][4 + jj] = v;
      }
    };
    split(0);
    __builtin_amdgcn_sched_barrier(0);
    #pragma unroll
    for (int m = 0; m < 8; ++m) {
      unsigned ro = (unsigned)(64 * (4 * wh + 2) + 32 * (m >> 2) + 16 * ((m >> 1) & 1) + 2 * (m & 1)) * 8192u;
      qa[2][m] = ld128(rq, voq, ro); qb[2][m] = ld128(rq, voq, ro + 8u * 8192u);
    }
    __builtin_amdgcn_sched_barrier(0);
    split(1);
    __builtin_amdgcn_sched_barrier(0);
    { const unsigned cs = beat_wait(rw, g0, g1, c, dead); if ((cs & 7u) != c) return cs; }
    {
      u32x4 ones = {~0u, ~0u, ~0u, ~0u};
      #pragma unroll
      for (int i = 0; i < 12; ++i) st128c(ones, rw, (unsigned)tid * 16u + (i & 3) * 4096u, (i < 8 ? OFF_PART + (i >> 2) * (1u << 20) : OFF_P2) + r * 16384u);
      #pragma unroll
      for (int i = 0; i < 4; ++i) {
        int s = 1 + (tid >> 3) + 32 * (i & 1);
        if (s < 64) st128c(ones, rw, mem_step(s) + (tid & 7) * 16u, mpc0 + (i >> 1) * 8192u);
      }
    }
    pw = ldflag(rw, FL_W);
    __builtin_amdgcn_sched_barrier(0);
    #pragma unroll
    for (int m = 0; m < 8; ++m) {
      unsigned ro = (unsigned)(64 * (4 * wh + 3) + 32 * (m >> 2) + 16 * ((m >> 1) & 1) + 2 * (m & 1)) * 8192u;
      qa[3][m] = ld128(rq, voq, ro); qb[3][m] = ld128(rq, voq, ro + 8u * 8192u);
    }
    __builtin_amdgcn_sched_barrier(0);
    #pragma unroll
    for (int jj = 0; jj < 2; ++jj)
      #pragma unroll
      for (int t = 0; t < 4; ++t)
        #pragma unroll
        for (int q = 0; q < 2; ++q) {
          u32x4 o;
          #pragma unroll
          for (int i = 0; i < 4; ++i) { int m = 4 * q + i; o[i] = (t & 1) ? phi(va[jj][m][t >> 1], vb[jj][m][t >> 1]) : plo(va[jj][m][t >> 1], vb[jj][m][t >> 1]); }
          *(u32x4*)(smem + 8192 + ((t * 8 + 2 * w + jj) * 2 + q) * 1024 + l * 16) = o;
        }
    __builtin_amdgcn_sched_barrier(0);
    u32x4 oa[8], ob[8];
    #pragma unroll
    for (int m = 0; m < 8; ++m) {
      unsigned ro = (unsigned)(32 * (m & 1) + 8 * ((m >> 1) & 1) + 2 * (m >> 2)) * 1024u;
      oa[m] = ld128(ro_, voo, ro); ob[m] = ld128(ro_, voo, ro + 4096u);
    }
    asm volatile("s_waitcnt vmcnt(32)" ::: "memory");
    bar();
    beat_ack(rw, tid);
    pw = pw_wait(rw, pw, dead);
    if (w == 0 && l < 32 && n < 8) {
      int i = 4 * gb + (n >> 1), cc = n & 1;
      int db = 128 * (r >> 4) + 32 * ((r >> 2) & 3) + 4 * ((r >> 1) & 1) + 2 * (r & 1) + cc;
      const uint16_t* mp = p.mem0 + i * 512 + db;
      u32x2 v = {guard((unsigned)mp[0] | ((unsigned)mp[8] << 16)), guard((unsigned)mp[16] | ((unsigned)mp[24] << 16))};
      st64c(v, rw, 64u * gb + 8u * n, OFF_MEM + mpc0 + (pw & 2u) * 4096u);
    }
    fetch(0); fetch(1);
    bar();
    split(2); split(3);
    __builtin_amdgcn_sched_barrier(0);
    kf0 = ld32c(rw, (unsigned)(l & 1) * (FL_V - FL_K) * 128u, OFF_FLAGS + (unsigned)(FL_K + h) * 128u);
    bar();
    fetch(2); fetch(3);
    __builtin_amdgcn_sched_barrier(0);
    #pragma unroll
    for (int j = 0; j < 8; ++j) {
      u32x4 a = *(const u32x4*)(smem + 8192 + ((w * 8 + (j ^ (4 * wh))) * 2) * 1024 + l * 16);
      u32x4 b = *(const u32x4*)(smem + 8192 + ((w * 8 + (j ^ (4 * wh))) * 2 + 1) * 1024 + l * 16);
      Wv[j] = (u32x8){a[0], a[1], a[2], a[3], b[0], b[1], b[2], b[3]};
      pin_v(Wv[j]);
    }
  { const int a = (int)__builtin_amdgcn_readlane(kf0, 0), b = (int)__builtin_amdgcn_readlane(kf0, 1); kvp = a < b ? a : b; }
  if (kvp < 1) {
    kvp = wait_flags(rw, FL_K + h, 1, 1, l, dead);
    const int t = wait_flags(rw, FL_V + h, 1, 1, l, dead); kvp = t < kvp ? t : kvp;
  }
  bar();
  setm0(pfm);
  NOP(0);
  ldlds<0>((unsigned)l * 16u, rsw, pfb[0] - 8192u); ldlds<1024>((unsigned)l * 16u, rsw, pfb[1] - 8192u); ldlds<2048>((unsigned)l * 16u, rsw, pfb[2] - 8192u);
    __builtin_amdgcn_sched_barrier(0);
    #pragma unroll
    for (int m = 0; m < 8; ++m)
      #pragma unroll
      for (int t = 0; t < 8; ++t) Wo[t][m] = (t & 1) ? phi(oa[m][t >> 1], ob[m][t >> 1]) : plo(oa[m][t >> 1], ob[m][t >> 1]);
    #pragma unroll
    for (int j = 0; j < 8; ++j) pin_v(Wo[j]);
  }
  pw = rfl(pw);
  const unsigned polA = (pw & 1u) * 8192u, polB = (pw & 2u) * 4096u, mpc = mpc0 + polB, polC = polA ^ RLX;
  const unsigned rfl_o = OFF_RF + polC + (unsigned)r * 128u;
  const int idx = (l & 8) ? 0xEEEE : 0x4444;
  const unsigned l16 = (unsigned)l * 16u;
  const int hi = gb >> 1, g1 = gb & 1;
  char* const qimg_w = smem + ((w >> 1) * 8 + 4 * g1) * 288 + 128 * (w & 1) + 64 * hi + 4 * n;
  const char* const qimg_r = smem + n * 288 + gb * 16;
  char* const oimg_w = smem + 4608 + n * 192 + 32 * w + 8 * gb;
  const char* const oimg_r = smem + 4608 + (l & 7) * 192 + (l >> 3) * 16;
  const unsigned pst = (unsigned)(16 * w + 4 * (n >> 2) + 2 * hi) * 16384u + r * 128u + (4 * g1 + (n & 3)) * 16u;
  const unsigned cho = (unsigned)(w & 1) * 4096u;
  TSR(1);
  f32x4 ninf;
  { float v = l < 32 ? -INFINITY : 0.f; ninf = (f32x4){v, v, v, v}; asm volatile("" : "+v"(ninf)); }
  u32x4 one4 = {0x3F803F80u, 0x3F803F80u, 0x3F803F80u, 0x3F803F80u};
  asm volatile("" : "+v"(one4));
  char* const kxl = smem + 8192 + l * 16;
  const unsigned ach = (unsigned)((w & 1) + (w >> 1) * 4) * 1024u;
  const unsigned agl = polB + (unsigned)((w & 1) * 2048 + (w >> 1) * 16384), lma = (unsigned)((l >> 3) * 256 + (l & 7) * 16);
  const char* const kst_r = smem + (8 + (l & 7)) * 288 + (2 * eh + ((l >> 3) & 1)) * 64 + gb * 16;
  const unsigned kst_o = w == 1 ? (unsigned)(((l >> 3) & 1) * 512 + gb * 128 + (l & 7) * 16 + eh * 1024) : 0x80000000u;
  unsigned slot = OFF_PART + polA, slotp = OFF_P2 + polB, slotn = OFF_PART + polA + (1u << 20);
  vm0();
  bar();
  unsigned cpv = (unsigned)__builtin_amdgcn_s_memtime(), rpv = rtc(), kf = 0u;
  int ctl = EXT0;
  TSR(2);
  if (w < 2) TSOUT(r * 2 + w, 64);
  auto step = [&](const int s, auto last_c) {
    constexpr bool LAST = decltype(last_c)::value;
    u32x4 A[8], Kx[8], Vx;
    f32x4 av;
    u32x2 lt;
    const unsigned mo = mem_step(s) + agl;
    int it = 0;
    TSR(0);
    TSK(1);
    setm0(ABUF + ach);
    NOP(0);
    ldlds<0>(lma, rsw, mo); ldlds<2048>(lma, rsw, mo + 2048u);
    if (!LAST) {
      #pragma unroll
      for (int i = 0; i < 8; ++i) Kx[i] = *(const u32x4*)(kxl + i * 1024);
      Vx = *(const u32x4*)(kxl + 8192 + w * 1024);
    }
    SB();
    unsigned ab = 0u;
    while (true) {
      const unsigned lb = lau(l16) + ab;
      const char* const a0 = smem + ABUF + cho + lb;
      const char* const a1 = smem + ABUF + (4096u - cho) + lb;
      unsigned mx = lau(0u);
      f32x4 c0 = zero4(), c1 = zero4(), c2 = zero4(), c3 = zero4();
      av = zero4();
      u32x2 o0, o1;
      unsigned rfv = (unsigned)(s - 1), rfo = tid == 64 ? 0u : 0x80000000u;
      asm volatile("" : "+v"(rfv), "+v"(rfo));
      ab ^= 8192u;
      const unsigned m0b = ABUF + ach + ab;
      if (!LAST) {
        setm0(pfm);
        asm volatile("v_mov_b64 %0, -1\n\tv_mov_b64 %1, -1" : "=v"(o0), "=v"(o1));
      }
      SB();
      asm volatile("s_waitcnt vmcnt(1) lgkmcnt(0)\n\ts_barrier" ::: "memory");
      A[0] = *(const u32x4*)a0; A[1] = *(const u32x4*)(a0 + 1024); A[4] = *(const u32x4*)a1; A[5] = *(const u32x4*)(a1 + 1024);
      SB();
      asm volatile("s_waitcnt vmcnt(0)\n\ts_barrier" ::: "memory");
      A[2] = *(const u32x4*)(a0 + 2048); A[3] = *(const u32x4*)(a0 + 3072); A[6] = *(const u32x4*)(a1 + 2048); A[7] = *(const u32x4*)(a1 + 3072);
      SB();
      #pragma unroll
      for (int jj = 0; jj < 8; ++jj) {
        const int j = (jj & 1) | ((jj & 2) << 1) | ((jj & 4) >> 1);
        smf4a(c0, c2, c1, c3, mx, A[j], Wqk[0][j], Wqk[2][j], Wqk[1][j], Wqk[3][j], idx);
        if (!LAST) {
          if (jj == 0) ldlds<0>(l16, rsw, pfb[0] + s * 8192u);
          if (jj == 1) ldlds<1024>(l16, rsw, pfb[1] + s * 8192u);
          if (jj == 2) ldlds<2048>(l16, rsw, pfb[2] + s * 8192u);
          if (jj == 3 || jj == 4) { SB(); st128c((u32x4){o0[0], o0[1], o1[0], o1[1]}, rw, l16, slotp + (unsigned)r * 16384u + w * 2048u + (jj == 4) * 1024u); SB(); }
        }
        if (jj == 3) setm0(m0b);
        if (jj == BJ) ldlds<0>(lma, rsw, mo);
        if (jj == BJ + 1) ldlds<2048>(lma, rsw, mo + 2048u);
        if (jj == RJ) { SB(); st32c(rfv, rw, rfo, rfl_o); SB(); }
      }
      SB();
      const f32x4 t0 = hsum4(c0, c2);
      SB();
      const bool ok = __builtin_amdgcn_ballot_w64(mx == 0xFFFFFFFFu) == 0ull;
      NOP(0);
      SB();
      const f32x4 t1 = hsum4(c1, c3);
      #pragma unroll
      for (int q = 0; q < 4; ++q) *(unsigned*)(qimg_w + q * 288) = pk(t0[q], t1[q]);
      asm volatile("" ::: "memory");
      if (ok) break;
      if (dead || ++it > SPIN_MAX) { dead = 1; break; }
    }
    TSK(2); TSV(10, it);
    if (!LAST) lt = ld64c(rw, 0u, w == 0 ? OFF_RL + polC + s * 32u : 0x80000000u);
    bar();
    TSK(3);
    u32x4 F[4];
    #pragma unroll
    for (int k = 0; k < 4; ++k) F[k] = *(const u32x4*)(qimg_r + k * 64);
    const u32x4 Kst = *(const u32x4*)kst_r;
    const unsigned kvmo = OFF_KVM + (kvb + s) * 4096u, vsto = kvmo + 2048u + eh * 1024u + w * 256u;
    SB();
    smfchain(av, A, Wv, idx);
    if (LAST) {
      st128c(Kst, rw, kst_o, kvmo);
      mwait(av);
      av = hsum4(av, av);
      st64c((u32x2){pk(av[0], av[1]), pk(av[2], av[3])}, rw, (l16 >> 1) & 0xF8u, vsto);
      vm0();
      bar();
      if (tid == 64) st32c(63u, rw, 0u, rfl_o);
      return;
    } else {
      f32x4 S0, S1, SM;
      mf0(S0, Kx[0], F[0]); mf0(S1, Kx[4], F[0]);
      float a0 = av[0], a1 = av[1], a2 = av[2], a3 = av[3];
      float b0 = vmov(a0), b1 = vmov(a1);
      mfc(SM, F[0], F[0], ninf);
      float b2 = vmov(a2), b3 = vmov(a3);
      mfa(S0, Kx[1], F[1]);
      swp(a0, b0); swp(a1, b1);
      mfa(S1, Kx[5], F[1]);
      swp(a2, b2); swp(a3, b3);
      mfa(SM, F[1], F[1]);
      a0 = vadd(a0, b0); a1 = vadd(a1, b1);
      mfa(S0, Kx[2], F[2]);
      a2 = vadd(a2, b2); a3 = vadd(a3, b3);
      mfa(S1, Kx[6], F[2]);
      const unsigned v0 = vcvt(a0, a1), v1 = vcvt(a2, a3);
      mfa(SM, F[2], F[2]);
      SB();
      st128c(Kst, rw, kst_o, kvmo);
      SB();
      const f32x2 zv = zero2(), zp = zero2();
      mfa(S0, Kx[3], F[3]);
      f32x4 Y[8];
      Y[0] = zero4();
      mfa(S1, Kx[7], F[3]);
      Y[4] = zero4();
      mfa(SM, F[3], F[3]);
      SB();
      st64c((u32x2){v0, v1}, rw, (l16 >> 1) & 0xF8u, vsto);
      SB();
      Y[1] = zero4(); Y[5] = zero4(); Y[2] = zero4();
      const u32x4 vm4 = {v0, v1, asu(zv[0]), asu(zv[1])};
      const float nm = negmax12(S0, S1, SM);
      #pragma unroll
      for (int q = 0; q < 4; ++q) {
        S0[q] = __builtin_amdgcn_exp2f(__builtin_fmaf(S0[q], SM_SCALE, nm));
        S1[q] = __builtin_amdgcn_exp2f(__builtin_fmaf(S1[q], SM_SCALE, nm));
        SM[q] = __builtin_amdgcn_exp2f(__builtin_fmaf(SM[q], SM_SCALE, nm));
      }
      u32x4 Px = {pk(S0[0], S0[1]), pk(S0[2], S0[3]), pk(S1[0], S1[1]), pk(S1[2], S1[3])};
      u32x4 Pm = {pk(SM[0], SM[1]), pk(SM[2], SM[3]), asu(zp[0]), asu(zp[1])};
      f32x4 O, L;
      pvblock(L, O, Y[6], Y[3], one4, Px, Pm, Vx, vm4);
      float inv;
      asm volatile("v_rcp_f32 %0, %1" : "=v"(inv) : "v"(L[0]));
      Y[7] = zero4();
      asm volatile("s_nop 4" : "+v"(O), "+v"(inv));
      O = O * inv;
      *(u32x2*)oimg_w = (u32x2){pk(O[0], O[1]), pk(O[2], O[3])};
      bar();
      TSK(4);
      u32x4 Ao = *(const u32x4*)oimg_r;
      smf1(Y[0], Ao, Wo[0], idx); smf1(Y[4], Ao, Wo[4], idx); smf1(Y[1], Ao, Wo[1], idx); smf1(Y[5], Ao, Wo[5], idx);
      float p[8], q[8];
      #pragma unroll
      for (int i = 0; i < 4; ++i) { p[i] = Y[0][i]; q[i] = Y[4][i]; }
      swp(p[0], q[0]); swp(p[1], q[1]);
      smf1(Y[2], Ao, Wo[2], idx);
      swp(p[2], q[2]); swp(p[3], q[3]);
      smf1(Y[6], Ao, Wo[6], idx);
      p[0] = vadd(p[0], q[0]); p[1] = vadd(p[1], q[1]);
      smf1(Y[3], Ao, Wo[3], idx);
      p[2] = vadd(p[2], q[2]); p[3] = vadd(p[3], q[3]);
      smf1(Y[7], Ao, Wo[7], idx);
      SB();
      {
        const f32x4 t1 = hsum4(Y[1], Y[5]);
        st128c((u32x4){pk(p[0], t1[0]), pk(p[1], t1[1]), pk(p[2], t1[2]), pk(p[3], t1[3])}, rw, pst, slot);
      }
      SB();
      {
        const f32x4 t0 = hsum4(Y[2], Y[6]), t1 = hsum4(Y[3], Y[7]);
        st128c((u32x4){pk(t0[0], t1[0]), pk(t0[1], t1[1]), pk(t0[2], t1[2]), pk(t0[3], t1[3])}, rw, pst, slot + 16384u);
      }
    }
    SB();
    const unsigned tps = rtc();
    TSK(5);
    if (w != 0) {
      if (w == 1 && kvp < 63 && kvp < s + 6) {
        const int need = s < 61 ? s + 2 : 63;
        int itf = 0;
        while (true) {
          const unsigned f = ld32c(rw, (lau(l16) & 16u) * 256u, OFF_FLAGS + (unsigned)h * 128u);
          const int a = (int)__builtin_amdgcn_readlane(f, 0), b = (int)__builtin_amdgcn_readlane(f, 1);
          kvp = a < b ? a : b;
          if (kvp >= need) break;
          if (dead || ++itf > SPIN_MAX) { dead = 1; break; }
        }
      }
      vm0();
    }
    SB();
    if (w == 0) {
      const unsigned so = slot + (unsigned)r * 16384u;
      const unsigned l0 = rfl(lt[0]), l1 = rfl(lt[1]);
      const bool lok = l1 < 256u && tps - l0 + 1024u < 16384u;
      const unsigned xm = lok ? l1 : XM0, rg = lok ? xm - RGD : RG0;
      const unsigned tP0 = lok ? l0 : tps + D1;
      TSK(6);
      unsigned tP = grid_wait(tP0, rg, TOL1, tps);
      TSK(7);
      f32x4 D;
      unsigned mso;
      int it2 = 0;
      while (true) {
        u32x4 P[8];
        const unsigned lb = lau(l16);
        #pragma unroll
        for (int j = 0; j < 8; ++j) P[j] = ld128c(rw, lb + (j & 3) * 1024u, so + (j >> 2) * 4096u);
        SB();
        u32x4 sel;
        #pragma unroll
        for (int q = 0; q < 4; ++q) sel[q] = ((lb >> 5) & 3u) == (unsigned)q ? ((lb & 16u) ? 0x3F800000u : 0x00003F80u) : 0u;
        mso = (lb & 0x280u) == 0u ? ((lb >> 2) & 64u) | ((lb >> 1) & 56u) : 0x80000000u;
        asm volatile("" : "+v"(sel), "+v"(mso));
        if (it2 == 0) {
          const unsigned cn = (unsigned)__builtin_amdgcn_s_memtime(), rn = rtc();
          const float k = (float)(rn - rpv) * __builtin_amdgcn_rcpf((float)(cn - cpv));
          kf = rfl(asu(s < 2 ? k : 0.75f * asf(kf) + 0.25f * k));
          cpv = cn; rpv = rn;
        }
        unsigned mx = lau(0u);
        D = zero4();
        NOP(1);
        SB();
        #pragma unroll
        for (int j = 0; j < 8; ++j) mf32_acc(D, mx, P[j], sel);
        SB();
        const bool ok = __builtin_amdgcn_ballot_w64(mx == 0xFFFFFFFFu) == 0ull;
        asm volatile("" ::: "memory");
        if (ok) break;
        if (dead || ++it2 > SPIN_MAX) { dead = 1; break; }
        tP = grid_wait(tP + rg, rg, TOL, rtc());
      }
      {
        float e0 = D[0], e1 = D[1], e2 = D[2], e3 = D[3], f0, f1, f2, f3;
        asm volatile("s_nop 3\n\tv_mov_b32 %4, %0\n\tv_mov_b32 %5, %1\n\tv_mov_b32 %6, %2\n\tv_mov_b32 %7, %3\n\t"
                     "v_permlane32_swap_b32 %0, %4\n\tv_permlane32_swap_b32 %1, %5\n\tv_permlane32_swap_b32 %2, %6\n\tv_permlane32_swap_b32 %3, %7\n\t"
                     "v_add_f32 %0, %0, %4\n\tv_add_f32 %1, %1, %5\n\tv_add_f32 %2, %2, %6\n\tv_add_f32 %3, %3, %7"
                     : "+v"(e0), "+v"(e1), "+v"(e2), "+v"(e3), "=&v"(f0), "=&v"(f1), "=&v"(f2), "=&v"(f3));
        st64c((u32x2){pk(e0, e1), pk(e2, e3)}, rw, mso, mem_step(s + 1) + mpc);
      }
      SB();
      TSK(8); TSV(11, it2);
      *(u32x2*)(smem + 7168) = (u32x2){tP + xm, rg};
      if (r == 0) {
        int ext = ctl & 255, fl = (ctl >> 8) & 255, miss = 0;
        if (lok && (it > 0 || tP != tP0)) {
          if (s >= FS0 && !(ctl >> 16)) fl = ext + FUP > FMAX ? FMAX : ext + FUP;
          ext += (ctl >> 16) ? ECAS : EUP;
          ext = ext > EMAX ? EMAX : ext;
          miss = 1;
        } else if (ext > fl) --ext;
        ctl = ext | (fl << 8) | (miss << 16);
        const unsigned yn = (unsigned)(asf(kf) * (s < 30 ? PERC - XMC : PERC - XMC - P2D) + 0.5f) + (ext - (ext >> 1)), xn = (unsigned)(asf(kf) * XMC + 0.5f) + (ext >> 1);
        if (l == 0) st64c((u32x2){tP + xm + yn + (lok ? 0u : Y0X), xn}, rw, 0u, OFF_RL + polC + (s + 1) * 32u);
      }
      SB();
    }
    bar();
    TSK(9);
    const unsigned tc = rtc();
    const u32x2 pace = *(const u32x2*)(smem + 7168);
    if (s >= RIM && tid == 64) st32c((unsigned)s, rw, 0u, rfl_o);
    { const unsigned t = slotp; slotp = slot; slot = slotn; slotn = t; }
    SB();
    grid_wait(rfl(pace[0]), rfl(pace[1]), TOLM, tc);
  };
  for (int s = 0; s < 63; ++s) step(s, BoolC<false>());
  step(63, BoolC<true>());
  return c;
}

template <int MODE>
DEVI unsigned role_proj(const Params& p, int u, bool again, unsigned gs, unsigned c, int wv, char* smem) {
  const int l = lane_id(), w = wv, tid = w * 64 + l;
  const int n = l & 15, g = l >> 4;
  rsrc_t rw = mkrsrc(p.ws, WS_BYTES);
  const unsigned g0 = again ? gs : ldbeat(rw);
  rsrc_t rx = mkrsrc(p.x, 2048u * 1024u);
  int dead = 0;
  TSDECL;
  TSR(0);
  if (MODE >= 2) for (int i = 0; i < DQ; ++i) __builtin_amdgcn_s_sleep(64);
  const unsigned xg = (unsigned)(8 * w + (l >> 3)) * 1024u + (unsigned)((l & 7) ^ (l >> 3)) * 16u;
  u32x4 X[8];
  #pragma unroll
  for (int i = 0; i < 8; ++i) X[i] = ld128(rx, xg + i * 128u, MODE == 2 ? 63u * 32768u : 0u);
  u32x4 W[2][16];
  {
    const unsigned ld = (MODE == 3) ? 3584u : 8192u;
    rsrc_t rm = mkrsrc(MODE == 0 ? p.wk : MODE == 1 ? p.wv : MODE == 2 ? p.wq : p.w1, 512u * ld);
    const unsigned vo = (unsigned)(8 * g) * ld + (unsigned)u * 256u + (MODE == 1 ? n * 8u : n * 16u);
    u32x4 a[4][8];
    #pragma unroll
    for (int i = 0; i < 4; ++i)
      #pragma unroll
      for (int j = 0; j < 8; ++j) {
        unsigned so = (unsigned)(32 * (4 * w + i) + j) * ld;
        if (MODE == 1) { u32x2 c = ld64(rm, vo, so), d = ld64(rm, vo + 128u, so); a[i][j] = (u32x4){c[0], c[1], d[0], d[1]}; }
        else a[i][j] = ld128(rm, vo, so);
      }
    const unsigned g1 = ldbeat(rw);
    __builtin_amdgcn_sched_barrier(0);
    #pragma unroll
    for (int i = 0; i < 4; ++i) {
      #pragma unroll
      for (int t = 0; t < 8; ++t) {
        u32x4 o;
        #pragma unroll
        for (int q = 0; q < 4; ++q) o[q] = (t & 1) ? phi(a[i][2 * q][t >> 1], a[i][2 * q + 1][t >> 1]) : plo(a[i][2 * q][t >> 1], a[i][2 * q + 1][t >> 1]);
        *(u32x4*)(smem + (t * 16 + 4 * w + i) * 1024 + l * 16) = o;
      }
    }
    { const unsigned cs = beat_wait(rw, g0, g1, c, dead); if ((cs & 7u) != c) return cs; }
    bar();
    beat_ack(rw, tid);
    #pragma unroll
    for (int t = 0; t < 2; ++t)
      #pragma unroll
      for (int k = 0; k < 16; ++k) {
        W[t][k] = *(const u32x4*)(smem + ((2 * w + t) * 16 + k) * 1024 + l * 16);
        if (MODE == 2) asm volatile("" : "+a"(W[t][k]));
      }
    bar();
  }
  float bias[2][4];
  if (MODE == 3) {
    #pragma unroll
    for (int q = 0; q < 4; ++q)
      #pragma unroll
      for (int t = 0; t < 2; ++t) bias[t][q] = asf((unsigned)p.b1[128 * u + 8 * (4 * g + q) + 2 * w + t] << 16);
  }
  const int xr0 = (n >> 3) * 8192 + (n & 7) * 128 + ((g ^ (n & 7)) * 16);
  const int xr1 = (n >> 3) * 8192 + (n & 7) * 128 + (((4 + g) ^ (n & 7)) * 16);
  const int flag = (MODE == 0 ? FL_K : MODE == 1 ? FL_V : MODE == 2 ? FL_O : FL_H) + u;
  int kvp = -1, rp = -1;
  unsigned rfo = 0u;
  if (MODE == 2) rfo = OFF_RF + (((pw_wait(rw, ldflag(rw, FL_W), dead) & 1u) * 8192u) ^ RLX) + (unsigned)u * 256u;
  TSR(1);
  if (w == 0 && u < 4) TSOUT(128 + MODE * 4 + u, 64);
    auto ldkvm = [&](u32x4* Km, u32x2* Vm, int s, unsigned skip) {
      const unsigned hs = (unsigned)(u * 64 + s);
      #pragma unroll
      for (int k = 0; k < 4; ++k) Km[k] = ld128c(rw, (unsigned)(g * 128 + (n & 7) * 16 + k * 512), (OFF_KVM + hs * 4096u) | skip);
      #pragma unroll
      for (int i = 0; i < 4; ++i) Vm[i] = ld64c(rw, (unsigned)((l & 31) * 8 + i * 256), (OFF_KVM + hs * 4096u + 2048u + (w >> 1) * 1024u) | skip);
    };
  auto attend = [&](int s, const char* qb, const u32x4* Kx, const u32x4* Vx, u32x4* Km, u32x2* Vm, bool early, unsigned f0) {
      const int TT = w & 1, eh = w >> 1;
      u32x4 Q[4];
      #pragma unroll
      for (int k = 0; k < 4; ++k) Q[k] = *(const u32x4*)(qb + (TT * 4 + k) * 1024 + l * 16);
      f32x4 S0 = {0.f, 0.f, 0.f, 0.f}, S1 = S0, SM = S0;
      #pragma unroll
      for (int k = 0; k < 4; ++k) { S0 = mf32(Kx[k], Q[k], S0); S1 = mf32(Kx[4 + k], Q[k], S1); }
      #pragma unroll
      for (int q = 0; q < 4; ++q) {
        if (4 * g + q > 16 * TT + n) S0[q] = -INFINITY;
        if (16 + 4 * g + q > 16 * TT + n) S1[q] = -INFINITY;
      }
      TSR(1);
      {
        asm volatile("" : "+v"(f0), "+v"(S0), "+v"(S1) :: "memory");
        { const int a = (int)__builtin_amdgcn_readlane(f0, 0), b = (int)__builtin_amdgcn_readlane(f0, 1); rp = a < b ? a : b; }
        int itf = 0;
        while (rp < s) {
          if (dead || ++itf > SPIN_MAX) { dead = 1; break; }
          if (QSL) __builtin_amdgcn_s_sleep(QSL);
          const unsigned fr = ld32c(rw, (lau((unsigned)l * 16u) & 16u) * 8u, rfo);
          const int a = (int)__builtin_amdgcn_readlane(fr, 0), b = (int)__builtin_amdgcn_readlane(fr, 1);
          rp = a < b ? a : b;
        }
      }
      TSR(2);
      if (!early) { ldkvm(Km, Vm, s, 0u); __builtin_amdgcn_s_waitcnt(0x0F70); }
      #pragma unroll
      for (int k = 0; k < 4; ++k) SM = mf32(Km[k], Q[k], SM);
      if (l < 32) SM = (f32x4){-INFINITY, -INFINITY, -INFINITY, -INFINITY};
      float m = fmaxf(fmaxf(fmaxf(S0[0], S0[1]), fmaxf(S0[2], S0[3])), fmaxf(fmaxf(S1[0], S1[1]), fmaxf(S1[2], S1[3])));
      m = fmaxf(m, fmaxf(fmaxf(SM[0], SM[1]), fmaxf(SM[2], SM[3])));
      m = red_max_g(m) * SM_SCALE;
      #pragma unroll
      for (int q = 0; q < 4; ++q) {
        S0[q] = __builtin_amdgcn_exp2f(__builtin_fmaf(S0[q], SM_SCALE, -m));
        S1[q] = __builtin_amdgcn_exp2f(__builtin_fmaf(S1[q], SM_SCALE, -m));
        SM[q] = __builtin_amdgcn_exp2f(__builtin_fmaf(SM[q], SM_SCALE, -m));
      }
      u32x4 Px = {pk(S0[0], S0[1]), pk(S0[2], S0[3]), pk(S1[0], S1[1]), pk(S1[2], S1[3])};
      u32x2 Pm = {pk(SM[0], SM[1]), pk(SM[2], SM[3])};
      f32x4 O[4];
      #pragma unroll
      for (int i = 0; i < 4; ++i) { O[i] = mf32(Vx[i], Px, (f32x4){0.f, 0.f, 0.f, 0.f}); O[i] = mf16(Vm[i], Pm, O[i]); }
      float sum = ((S0[0] + S0[1]) + (S0[2] + S0[3])) + ((S1[0] + S1[1]) + (S1[2] + S1[3])) + ((SM[0] + SM[1]) + (SM[2] + SM[3]));
      float inv = __builtin_amdgcn_rcpf(red_sum_g(sum));
      #pragma unroll
      for (int q = 0; q < 2; ++q) {
        u32x4 o = {pk(O[2 * q][0] * inv, O[2 * q][1] * inv), pk(O[2 * q][2] * inv, O[2 * q][3] * inv),
                   pk(O[2 * q + 1][0] * inv, O[2 * q + 1][1] * inv), pk(O[2 * q + 1][2] * inv, O[2 * q + 1][3] * inv)};
        st128c(o, rw, (unsigned)((n >> 3) * 65536 + q * 512 + g * 128 + (n & 7) * 16),
               OFF_OF + (unsigned)((s * 4 + 2 * TT) * 64 + 2 * u + eh) * 1024u);
      }
  };
  for (int s = MODE == 2 ? -1 : 0; s < (MODE == 2 ? 63 : 64); ++s) {
    TSR(0); TSK(4);
    char* xb = smem + (s & 1) * 32768;
    #pragma unroll
    for (int i = 0; i < 8; ++i) *(u32x4*)(xb + (w * 8 + i) * 1024 + l * 16) = X[i];
    TSK(5);
    if (MODE != 2) asm volatile("s_waitcnt vmcnt(2)" ::: "memory");
    TSK(6);
    bar();
    TSK(7);
    if (MODE != 2 && s >= 4 && tid == 0) st32c((unsigned)(s - 2), rw, (unsigned)flag * 128u, OFF_FLAGS);
    const unsigned xl = lau(xg), xso = (unsigned)(s < 63 ? s + 1 : s) * 32768u;
    const unsigned hs = (unsigned)(u * 64 + s), ll = lau((unsigned)l * 16u);
    u32x4 Kx[8], Vx[4];
    const unsigned kskip = s < 0 ? 0x80000000u : 0u;
    if (MODE == 2 && kvp < s) {
      kvp = wait_flags(rw, FL_K + u, 1, s, l, dead);
      int t = wait_flags(rw, FL_V + u, 1, s, l, dead); kvp = t < kvp ? t : kvp;
    }
    u32x4 Km[4]; u32x2 Vm[4];
    unsigned fr = 0;
    bool early = false;
    if (MODE == 2) {
      fr = ld32c(rw, (ll & 16u) * 8u, rfo);
    }
    SB();
    f32x4 D[2][2];
    #pragma unroll
    for (int t = 0; t < 2; ++t) { D[t][0] = (f32x4){0.f, 0.f, 0.f, 0.f}; D[t][1] = D[t][0]; }
    u32x4 xf[4][2];
    #pragma unroll
    for (int k = 0; k < 3; ++k) {
      xf[k][0] = *(const u32x4*)(xb + ((k & 1) ? xr1 : xr0) + (k >> 1) * 1024);
      xf[k][1] = *(const u32x4*)(xb + ((k & 1) ? xr1 : xr0) + (k >> 1) * 1024 + 16384);
    }
    SB();
    #pragma unroll
    for (int k = 0; k < 16; ++k) {
      if (MODE == 2 && k == QK2) {
        asm volatile("" : "+v"(fr) : "v"(D[0][0]), "v"(D[0][1]), "v"(D[1][0]), "v"(D[1][1]));
        const int a = (int)__builtin_amdgcn_readlane(fr, 0), b = (int)__builtin_amdgcn_readlane(fr, 1);
        asm volatile("" ::: "memory");
        early = (a < b ? a : b) >= (s < 0 ? 64 : s);
        ldkvm(Km, Vm, s, early ? 0u : 0x80000000u);
        fr = ld32c(rw, (lau(ll) & 16u) * 8u, rfo);
        SB();
      }
      #pragma unroll
      for (int T = 0; T < 2; ++T) {
        #pragma unroll
        for (int t = 0; t < 2; ++t) {
          if (MODE == 1) D[t][T] = mf32(xf[k & 3][T], W[t][k], D[t][T]);
          else D[t][T] = mf32(W[t][k], xf[k & 3][T], D[t][T]);
          if (t == 0 && k < 13) xf[(k + 3) & 3][T] = *(const u32x4*)(xb + (((k + 3) & 1) ? xr1 : xr0) + ((k + 3) >> 1) * 1024 + T * 16384);
          if (MODE != 2 && t == 1 && T == 1 && k < 8) X[k] = ld128(rx, xl + k * 128u, xso);
          if (MODE == 2 && t == 1 && T == 1 && k >= 8) X[k - 8] = ld128(rx, xl + (k - 8) * 128u, xso);
          if (MODE == 2 && t == 1 && k < 6) {
            const int j = 2 * k + T;
            if (j < 8) Kx[j] = ld128c(rw, ll + (j & 3) * 1024u, (OFF_KX + hs * 8192u + (j >> 2) * 4096u) | kskip);
            else Vx[j - 8] = ld128c(rw, ll + (j & 3) * 1024u, (OFF_VX + hs * 8192u + (w >> 1) * 4096u) | kskip);
          }
          SB();
        }
      }
    }
    asm volatile("" :: "v"(D[0][0]), "v"(D[0][1]), "v"(D[1][0]), "v"(D[1][1]));
    TSK(8);
    if (MODE == 0) {
      #pragma unroll
      for (int T = 0; T < 2; ++T) {
        u32x4 o = {pk(D[0][T][0], D[1][T][0]), pk(D[0][T][1], D[1][T][1]), pk(D[0][T][2], D[1][T][2]), pk(D[0][T][3], D[1][T][3])};
        st128c(o, rw, (unsigned)l * 16u, OFF_KX + (unsigned)(u * 64 + s) * 8192u + (T * 4 + w) * 1024u);
      }
    } else if (MODE == 1) {
      #pragma unroll
      for (int t = 0; t < 2; ++t) {
        u32x4 o = {pk(D[t][0][0], D[t][0][1]), pk(D[t][0][2], D[t][0][3]), pk(D[t][1][0], D[t][1][1]), pk(D[t][1][2], D[t][1][3])};
        st128c(o, rw, (unsigned)l * 16u, OFF_VX + (unsigned)(u * 64 + s) * 8192u + (2 * w + t) * 1024u);
      }
    } else if (MODE == 3) {
      #pragma unroll
      for (int T = 0; T < 2; ++T) {
        u32x4 o;
        #pragma unroll
        for (int q = 0; q < 4; ++q) {
          float a = fmaxf(D[0][T][q] + bias[0][q], 0.f), b = fmaxf(D[1][T][q] + bias[1][q], 0.f);
          o[q] = pk(a * a, b * b);
        }
        st128c(o, rw, (unsigned)((n >> 3) * 28 * 1024 + g * 128 + (n & 7) * 16),
               OFF_HF + (unsigned)((s * 4 + 2 * T) * 28 + 2 * u + (w >> 1)) * 1024u + (w & 1) * 512u);
      }
    } else {
      char* qb = smem + (s < 0 ? 73728 : 65536);
      #pragma unroll
      for (int T = 0; T < 2; ++T) {
        u32x4 o = {pk(D[0][T][0], D[1][T][0]), pk(D[0][T][1], D[1][T][1]), pk(D[0][T][2], D[1][T][2]), pk(D[0][T][3], D[1][T][3])};
        *(u32x4*)(qb + (T * 4 + w) * 1024 + l * 16) = o;
      }
      asm volatile("s_waitcnt vmcnt(30)" ::: "memory");
      bar();
      st32c((unsigned)(s - 1), rw, tid == 0 ? 0u : 0x80000000u, s > 0 ? OFF_FLAGS + (unsigned)flag * 128u : 0x40000000u);
      if (s >= 0) attend(s, qb, Kx, Vx, Km, Vm, early, fr);
    }
    if ((MODE != 2 && s < 2) || s == 63) {
      vm0();
      bar();
      if (tid == 0) st32c((unsigned)s, rw, (unsigned)flag * 128u, OFF_FLAGS);
    }
    TSR(3); TSK(9);
    if (w == 0 && u < 4 && s >= 0 && (MODE == 2 || s < 2 || s == 63)) TSOUT(128 + MODE * 4 + u, s);
  }
  if (MODE == 2) {
    u32x4 Kx[8], Vx[4], Km[4]; u32x2 Vm[4];
    const unsigned hs = (unsigned)(u * 64 + 63), ll = (unsigned)l * 16u;
    if (kvp < 63) { wait_flags(rw, FL_K + u, 1, 63, l, dead); wait_flags(rw, FL_V + u, 1, 63, l, dead); }
    #pragma unroll
    for (int k = 0; k < 8; ++k) Kx[k] = ld128c(rw, ll + (k & 3) * 1024u, OFF_KX + hs * 8192u + (k >> 2) * 4096u);
    #pragma unroll
    for (int k = 0; k < 4; ++k) Vx[k] = ld128c(rw, ll + k * 1024u, OFF_VX + hs * 8192u + (w >> 1) * 4096u);
    const unsigned fr = ld32c(rw, (ll & 16u) * 8u, rfo);
    asm volatile("s_waitcnt vmcnt(13)" ::: "memory");
    bar();
    st32c(62u, rw, tid == 0 ? 0u : 0x80000000u, OFF_FLAGS + (unsigned)flag * 128u);
    attend(63, smem + 73728, Kx, Vx, Km, Vm, false, fr);
    vm0();
    bar();
    if (tid == 0) st32c(63u, rw, (unsigned)flag * 128u, OFF_FLAGS);
  }
  return c;
}

DEVI unsigned role_gate(const Params& p, int u, bool again, unsigned gs, unsigned c, int wv) {
  const int l = lane_id(), w = wv, tid = w * 64 + l;
  rsrc_t rw = mkrsrc(p.ws, WS_BYTES);
  const unsigned g0 = again ? gs : ldbeat(rw);
  rsrc_t rx = mkrsrc(p.x, 2048u * 1024u);
  for (int i = 0; i < DG; ++i) __builtin_amdgcn_s_sleep(64);
  u32x4 gw = *(const u32x4*)(p.gw + l * 8);
  float mine = 0.f;
  for (int b = 0; b < 4; ++b) {
    u32x4 X[8];
    #pragma unroll
    for (int i = 0; i < 8; ++i) X[i] = ld128(rx, (unsigned)l * 16u, (unsigned)(u * 128 + w * 32 + b * 8 + i) * 1024u);
    #pragma unroll
    for (int i = 0; i < 8; ++i) {
      float a = 0.f;
      #pragma unroll
      for (int q = 0; q < 4; ++q) {
        a = __builtin_fmaf(asf(X[i][q] << 16), asf(gw[q] << 16), a);
        a = __builtin_fmaf(asf(X[i][q] & 0xFFFF0000u), asf(gw[q] & 0xFFFF0000u), a);
      }
      #pragma unroll
      for (int o = 1; o < 64; o <<= 1) a += __shfl_xor(a, o);
      if (l == b * 8 + i) mine = a;
    }
  }
  float gv = 1.f / (1.f + __expf(-mine));
  int dead = 0;
  { const unsigned cs = beat_wait(rw, g0, ldbeat(rw), c, dead); if ((cs & 7u) != c) return cs; }
  if (l < 32) st32c(asu(gv), rw, (unsigned)(u * 128 + w * 32 + l) * 4u, OFF_G);
  vm0();
  bar();
  beat_ack(rw, tid);
  if (tid == 0) st32c(0u, rw, (unsigned)(FL_G + u) * 128u, OFF_FLAGS);
  return c;
}

DEVI unsigned role_final(const Params& p, int G, int C, bool again, unsigned gs, unsigned c, int wv, char* smem) {
  const int l = lane_id(), w = wv, tid = w * 64 + l;
  const int n = l & 15, gb = l >> 4;
  rsrc_t rw = mkrsrc(p.ws, WS_BYTES);
  const unsigned g0 = again ? gs : ldbeat(rw);
  int dead = 0;
  TSDECL;
  TSR(0);
  for (int i = 0; i < DF; ++i) __builtin_amdgcn_s_sleep(64);
  { const unsigned cs = beat_wait(rw, g0, ldbeat(rw), c, dead); if ((cs & 7u) != c) return cs; }
  bar();
  beat_ack(rw, tid);
  u32x8 WA[16][2], WB[7][2];
  {
    rsrc_t r2 = mkrsrc(p.w2, 1792u * 1024u);
    const unsigned vo = (unsigned)(64 * (gb >> 1) + 16 * (gb & 1)) * 1024u + (32 * C + 2 * n) * 2u;
    #pragma unroll
    for (int i = 0; i < 7; ++i) {
      const int c = 7 * w + i;
      const unsigned cb = (unsigned)(128 * (c >> 1) + 4 * (c & 1)) * 1024u;
      unsigned a[8], b[8];
      #pragma unroll
      for (int m = 0; m < 8; ++m) {
        unsigned ro = cb + (unsigned)(32 * (m & 1) + 8 * ((m >> 1) & 1) + 2 * (m >> 2)) * 1024u;
        a[m] = ld32(r2, vo, ro); b[m] = ld32(r2, vo + 1024u, ro);
      }
      #pragma unroll
      for (int m = 0; m < 8; ++m) { WB[i][0][m] = plo(a[m], b[m]); WB[i][1][m] = phi(a[m], b[m]); }
      if (i & 1) __builtin_amdgcn_sched_barrier(0);
    }
    rsrc_t ro_ = mkrsrc(p.wo, 4096u * 1024u);
    const unsigned vo2 = (unsigned)(32 * (gb >> 1) + (gb & 1)) * 1024u + (32 * C + 2 * n) * 2u;
    #pragma unroll
    for (int i = 0; i < 16; ++i) {
      const unsigned cb = (unsigned)(64 * (16 * w + i)) * 1024u;
      unsigned a[8], b[8];
      #pragma unroll
      for (int m = 0; m < 8; ++m) {
        unsigned ro = cb + (unsigned)(16 * (m & 1) + 8 * ((m >> 1) & 1) + 2 * (m >> 2)) * 1024u;
        a[m] = ld32(ro_, vo2, ro); b[m] = ld32(ro_, vo2, ro + 4096u);
      }
      #pragma unroll
      for (int m = 0; m < 8; ++m) { WA[i][0][m] = plo(a[m], b[m]); WA[i][1][m] = phi(a[m], b[m]); }
      pin_a(WA[i][0]); pin_a(WA[i][1]);
      if (i & 1) __builtin_amdgcn_sched_barrier(0);
    }
  }
  const int idx = (l & 8) ? 0xEEEE : 0x4444;
  const unsigned l16 = (unsigned)l * 16u;
  const int oi = (tid >> 4) & 7, cp = tid & 15;
  const unsigned b2p = *(const unsigned*)(p.b2 + 32 * C + 2 * cp);
  wait_flags(rw, FL_G, 16, 0, l, dead);
  int hp = -1, op = -1;
  TSR(1);
  if (w == 0 && C == 0) TSOUT(144 + G, 64);
  f32x4 Bn;
  float gn;
  auto fpart = [&](int s) {
    const unsigned unit = (unsigned)(s * 4 + G);
    if (hp < s) hp = wait_flags(rw, FL_H, 14, s, l, dead);
    f32x4 B0 = {0.f, 0.f, 0.f, 0.f}, B1 = B0;
    u32x4 Hh[7];
    #pragma unroll
    for (int i = 0; i < 7; ++i) Hh[i] = ld128c(rw, l16 + (i & 3) * 1024u, OFF_HF + (unit * 28u + 7 * w) * 1024u + (i >> 2) * 4096u);
    gn = asf(ld32c(rw, (unsigned)oi * 4u, OFF_G + unit * 32u));
    #pragma unroll
    for (int i = 0; i < 7; ++i) smf2v(B0, B1, Hh[i], WB[i][0], WB[i][1], idx);
    mwait(B0, B1);
    Bn = hsum4(B0, B1);
  };
  fpart(0);
  for (int s = 0; s < 64; ++s) {
    TSR(0);
    const unsigned unit = (unsigned)(s * 4 + G);
    f32x4 A0 = {0.f, 0.f, 0.f, 0.f}, A1 = A0;
    const float gv = gn;
    char* red = smem + (s & 1) * 8192;
    char* rwp = red + w * 2048 + (16 * (gb >> 1) + n) * 32 + 16 * (gb & 1);
    *(f32x4*)(rwp + 1024) = Bn;
    if (s < 63) fpart(s + 1);
    TSR(1);
    if (op < s) {
      int it = 0;
      while (true) {
        const int v = (int)ld32c(rw, (unsigned)(l & 7) * 128u, OFF_FLAGS + (unsigned)(FL_O + 8 * w) * 128u);
        asm volatile("" ::: "memory");
        if (__builtin_amdgcn_ballot_w64(v < s) == 0ull) { op = __builtin_amdgcn_ballot_w64(v <= s) == 0ull ? s + 1 : s; break; }
        if (dead || ++it > SPIN_MAX) { dead = 1; break; }
        __builtin_amdgcn_s_sleep(PSL);
      }
    }
    TSR(2);
    {
      u32x4 Oo[16];
      #pragma unroll
      for (int i = 0; i < 16; ++i) Oo[i] = ld128c(rw, l16 + (i & 3) * 1024u, OFF_OF + (unit * 64u + 16 * w) * 1024u + (i >> 2) * 4096u);
      #pragma unroll
      for (int i = 0; i < 16; ++i) smf2a(A0, A1, Oo[i], WA[i][0], WA[i][1], idx);
      mwait(A0, A1);
    }
    *(f32x4*)rwp = hsum4(A0, A1);
    bar();
    if (tid < 128) {
      float a0 = 0.f, a1 = 0.f, b0 = asf(b2p << 16), b1 = asf(b2p & 0xFFFF0000u);
      #pragma unroll
      for (int q = 0; q < 4; ++q) {
        const float* rp = (const float*)(red + q * 2048 + cp * 32 + oi * 4);
        a0 += rp[0]; a1 += rp[128]; b0 += rp[256]; b1 += rp[384];
      }
      *(unsigned*)(p.out + (unit * 8u + oi) * 512u + 32 * C + 2 * cp) = pk(gv * a0 + (1.f - gv) * b0, gv * a1 + (1.f - gv) * b1);
    }
    TSR(3);
    if (w == 0 && C == 0) TSOUT(144 + G, s);
  }
  return c;
}

DEVI void role_reset(const Params& p, unsigned c, int wv) {
  const unsigned tid = (unsigned)(wv * 64 + lane_id());
  rsrc_t rw = mkrsrc(p.ws, WS_BYTES);
  const unsigned g0 = ldbeat(rw);
  const u32x4 ones = {~0u, ~0u, ~0u, ~0u};
  if (tid < FL_P) st32c(~0u, rw, tid * 128u, OFF_FLAGS);
  st32c(~0u, rw, tid * 128u, OFF_ACK);
  if (tid < 128) st32c(~0u, rw, tid * 128u, OFF_RF);
  st128c(ones, rw, (tid & 127u) * 16u + (tid >> 7) * 8192u, OFF_RL);
  #pragma unroll
  for (int i = 0; i < 4; ++i) st128c(ones, rw, (tid >> 3) * 256u + (tid & 7u) * 16u, OFF_MEM + i * 8192u);
  vm0();
  bar();
  if (wv != 0) return;
  unsigned g = rfl(g0) >> 3, pend = tid == 0 ? 14u : 15u;
  for (int it = 0; it < SPIN_MAX; ++it) {
    ++g;
    if (tid == 0) st32c((g << 3) | c, rw, 0u, OFF_FLAGS + FL_P * 128u);
    #pragma unroll
    for (int q = 0; q < 4; ++q)
      if ((pend >> q) & 1u) { if (ld32c(rw, (tid + 64u * q) * 128u, OFF_ACK) == 0u) pend &= ~(1u << q); }
    if (__builtin_amdgcn_ballot_w64(pend != 0u) == 0ull) break;
    __builtin_amdgcn_s_sleep(BSL);
    asm volatile("" ::: "memory");
  }
}
DEVI unsigned role_probe(const Params& p, bool again, unsigned gs, unsigned c, int wv, unsigned xcc) {
  const int tid = wv * 64 + lane_id();
  rsrc_t rw = mkrsrc(p.ws, WS_BYTES);
  const unsigned g0 = again ? gs : ldbeat(rw);
  const unsigned r0 = rtc();
  int dead = 0;
  const unsigned cs = beat_wait(rw, g0, ldbeat(rw), c, dead);
  if ((cs & 7u) != c) return cs;
  bar();
  beat_ack(rw, tid);
  unsigned best[4] = {~0u, ~0u, ~0u, ~0u};
  const int nrep = wv == 0 ? 64 : 0;
  for (int rep = 0; rep < nrep; ++rep) {
    #pragma unroll
    for (int k = 0; k < 4; ++k) {
      const unsigned t0 = (unsigned)__builtin_amdgcn_s_memtime();
      asm volatile("s_waitcnt lgkmcnt(0)" ::: "memory");
      st32c(~0u, rw, (unsigned)(tid & 31) * 4u, ((k & 2) ? OFF_P2 : OFF_PART) + (k & 1) * 8192u);
      vm0();
      const unsigned t1 = (unsigned)__builtin_amdgcn_s_memtime();
      asm volatile("s_waitcnt lgkmcnt(0)" ::: "memory");
      if (t1 - t0 < best[k]) best[k] = t1 - t0;
    }
    if (rep >= 2 && rtc() - r0 > PRT) break;
  }
  const unsigned pw = ((best[1] < best[0] ? 1u : 0u) | (best[3] < best[2] ? 2u : 0u)) ^ ((xcc & 4u) ? 3u : 0u) ^ PFLIP;
  if (tid == 0) st32c(pw, rw, 0u, OFF_FLAGS + FL_W * 128u);
  return c;
}

DEVI unsigned run_role(const Params& p, int b, bool again, unsigned gs, unsigned c, int wv, unsigned xcc, char* smem) {
  int x = (b + c) & 7, i = (b >> 3) - 1;
  if (i < 0) {
    const int j = x - (x > (int)c ? 1 : 0);
    x = j < 3 ? j : j + 1; i = 31;
  }
  if (x < 4) {
    if (i < 16) return role_rec(p, x * 16 + i, again, gs, c, wv, smem);
    if (i < 24) return role_proj<2>(p, x * 8 + (i - 16), again, gs, c, wv, smem);
    if (i < 28) return role_gate(p, x * 4 + (i - 24), again, gs, c, wv);
    if (i < 31 || x < 2) return role_proj<3>(p, i < 31 ? x * 3 + (i - 28) : 12 + x, again, gs, c, wv, smem);
    return role_probe(p, again, gs, c, wv, xcc);
  }
  const int y = x - 4;
  if (i < 8) return role_proj<0>(p, y * 8 + i, again, gs, c, wv, smem);
  if (i < 16) return role_proj<1>(p, y * 8 + (i - 8), again, gs, c, wv, smem);
  return role_final(p, y, i - 16, again, gs, c, wv, smem);
}

__global__ void __launch_bounds__(256, 1) gated_mix_kernel(Params p) {
  extern __shared__ __attribute__((aligned(16))) char smem[];
  const int b = blockIdx.x;
  unsigned xcc;
  asm volatile("s_getreg_b32 %0, hwreg(HW_REG_XCC_ID, 0, 4)" : "=s"(xcc));
  unsigned c = (xcc - (unsigned)b) & 7u;
  const int wv = (int)rfl(threadIdx.x >> 6);
  if (b == 0) { role_reset(p, c, wv); return; }
  const unsigned cs = rfl(run_role(p, b, false, 0u, c, wv, xcc, smem));
  if ((cs & 7u) != c) run_role(p, b, true, cs ^ 0x80000000u, cs & 7u, wv, xcc, smem);
}

extern "C" void kernel_launch(void* const* d_in, const int* in_sizes, int n_in,
                              void* d_out, int out_size, void* d_ws, size_t ws_size,
                              hipStream_t stream) {
  Params p;
  p.x = (const uint16_t*)d_in[0]; p.mem0 = (const uint16_t*)d_in[1];
  p.wq = (const uint16_t*)d_in[2]; p.wk = (const uint16_t*)d_in[3]; p.wv = (const uint16_t*)d_in[4]; p.wo = (const uint16_t*)d_in[5];
  p.w1 = (const uint16_t*)d_in[8]; p.b1 = (const uint16_t*)d_in[9]; p.w2 = (const uint16_t*)d_in[10]; p.b2 = (const uint16_t*)d_in[11];
  p.gw = (const uint16_t*)d_in[12];
  p.out = (uint16_t*)d_out;
  p.ws = (char*)(((uintptr_t)d_ws + (2u << 20) - 1) & ~(uintptr_t)((2u << 20) - 1));
  (void)hipFuncSetAttribute((const void*)gated_mix_kernel, hipFuncAttributeMaxDynamicSharedMemorySize, 139264);
  gated_mix_kernel<<<256, 256, 139264, stream>>>(p);
}
```
